```python
import torch

def _softmax(x, axis=-1):
    return torch.softmax(x, dim=axis)

def _gelu(x):
    return torch.nn.functional.gelu(x, approximate='tanh')

def _silu(x):
    return torch.nn.functional.silu(x)

def _sigmoid(x):
    return torch.sigmoid(x)

def _layernorm(x, axis=-1, eps=1e-05):
    m = x.mean(dim=axis, keepdim=True)
    v = x.var(dim=axis, keepdim=True, unbiased=False)
    return (x - m) / torch.sqrt(v + eps)

def _scatter_add(tgt, idx, src, axis=0):
    return tgt.clone().index_add_(axis, idx, src)

def _segment_sum(src, seg, n):
    out = torch.zeros((n,) + src.shape[1:], dtype=src.dtype)
    return out.index_add_(0, seg, src)

def _pad(x, pw):
    flat = tuple((v for pair in reversed(pw) for v in pair))
    return torch.nn.functional.pad(x, flat)
kernel_name = 'standalone__swin_window_attn__vision'

def setup_inputs():

    _w = lambda a: (lambda t: t.to(torch.bfloat16) if t.is_floating_point() else t)(torch.as_tensor(a))
    H = W = 224 // 7 * 7
    C, win = (192, 7)
    return {'x': _w((lambda *s: torch.randn(*s, dtype=torch.bfloat16) * 0.2)(1, H, W, C)), 'wqkv': _w((lambda *s: torch.randn(*s, dtype=torch.bfloat16) * 0.2)(C, 3 * C)), 'wo': _w((lambda *s: torch.randn(*s, dtype=torch.bfloat16) * 0.05)(C, C))}

def reference(x, wqkv, wo):
    B, H, W, C = x.shape
    win = 7
    nh, nw = (H // win, W // win)
    xw = (lambda x, *d: x.permute(*d))(x.reshape(B, nh, win, nw, win, C), 0, 1, 3, 2, 4, 5)
    xw = xw.reshape(B * nh * nw, win * win, C)
    qkv = xw @ wqkv
    q, k, v = (qkv[..., :C].clone(), qkv[..., C:2 * C].clone(), qkv[..., 2 * C:].clone())
    scale = C ** (-0.5)
    p = _softmax(torch.einsum('bqc,bkc->bqk', q, k) * scale, axis=-1)
    o = torch.einsum('bqk,bkc->bqc', p, v) @ wo
    o = (lambda x, *d: x.permute(*d))(o.reshape(B, nh, nw, win, win, C), 0, 1, 3, 2, 4, 5)
    return o.reshape(B, H, W, C)
```

```cpp
#include <hip/hip_runtime.h>
#include <stdint.h>

typedef __attribute__((ext_vector_type(8))) __bf16 bf16x8;
typedef __attribute__((ext_vector_type(4))) __bf16 bf16x4;
typedef __attribute__((ext_vector_type(4))) short s16x4;
typedef __attribute__((ext_vector_type(4))) float f32x4;
typedef __attribute__((ext_vector_type(4))) unsigned u32x4;
typedef __attribute__((ext_vector_type(2))) unsigned u32x2;
#define LAS __attribute__((address_space(3)))
#define DI __device__ __forceinline__
#define MFMA(a, b, c) __builtin_amdgcn_mfma_f32_16x16x32_bf16((a), (b), (c), 0, 0, 0)
#define SBAR() __builtin_amdgcn_sched_barrier(0)

constexpr int C = 192;
constexpr int ROWB = 384;
constexpr int IMG_ROWB = 224 * ROWB;
constexpr int LDS_X = 0;
constexpr int LDS_Y = 81920;
constexpr int LDS_P = LDS_Y + 24576;
constexpr int LDS_O = LDS_P + 8192;
constexpr int LDS_BYTES = LDS_O + 67 * ROWB;

DI int chmap(int w, int t, int idx) {
  const int g = idx >> 2, r = idx & 3;
  return t < 2 ? 32 * w + 8 * g + 4 * t + r : 128 + 32 * (w >> 1) + 8 * g + 4 * (w & 1) + r;
}

DI unsigned pack2(float a, float b) {
  typedef __attribute__((ext_vector_type(2))) __bf16 bf16x2;
  bf16x2 v; v[0] = (__bf16)a; v[1] = (__bf16)b;
  return __builtin_bit_cast(unsigned, v);
}

__global__ __launch_bounds__(64) void fold_kernel(const __bf16* __restrict__ wqkv, const __bf16* __restrict__ wo,
                                                  u32x4* __restrict__ wf) {
  __shared__ __attribute__((aligned(16))) char tr[192 * 32];
  const int f = blockIdx.x, lane = threadIdx.x;
  const int t = f % 3, s = (f / 3) % 6, w = (f / 18) & 3, mat = f / 72;
  const int idx = lane & 15, g = lane >> 4;
  const int c0 = 32 * s + 8 * (idx >> 2) + (idx & 3);
  const __bf16* ap = wqkv + (mat ? 2 * C : 0) + c0 * (3 * C) + 8 * g;
  bf16x8 a0[6], a1[6], b[6];
#pragma unroll
  for (int ks = 0; ks < 6; ++ks) {
    a0[ks] = *(const bf16x8*)(ap + 32 * ks);
    a1[ks] = *(const bf16x8*)(ap + 4 * 3 * C + 32 * ks);
  }
  if (mat == 0) {
    const __bf16* bp = wqkv + C + chmap(w, t, idx) * (3 * C) + 8 * g;
#pragma unroll
    for (int ks = 0; ks < 6; ++ks) b[ks] = *(const bf16x8*)(bp + 32 * ks);
  } else {
    const __bf16* bp = wo + (lane >> 2) * C + chmap(w, t, 4 * (lane & 3));
    u32x2 v[12];
#pragma unroll
    for (int n = 0; n < 12; ++n) v[n] = *(const u32x2*)(bp + 16 * n * C);
#pragma unroll
    for (int n = 0; n < 12; ++n) *(u32x2*)(tr + 512 * n + 8 * lane) = v[n];
    __syncthreads();
    const LAS char* tp = (const LAS char*)tr + (8 * g + (idx >> 2)) * 32 + 8 * (idx & 3);
#pragma unroll
    for (int ks = 0; ks < 6; ++ks) {
      s16x4 lo = __builtin_amdgcn_ds_read_tr16_b64_v4i16((LAS s16x4*)(tp + 1024 * ks));
      s16x4 hi = __builtin_amdgcn_ds_read_tr16_b64_v4i16((LAS s16x4*)(tp + 1024 * ks + 128));
      b[ks] = __builtin_bit_cast(bf16x8, __builtin_shufflevector(lo, hi, 0, 1, 2, 3, 4, 5, 6, 7));
    }
  }
  f32x4 d0 = {0.f, 0.f, 0.f, 0.f}, d1 = {0.f, 0.f, 0.f, 0.f};
#pragma unroll
  for (int ks = 0; ks < 6; ++ks) { d0 = MFMA(a0[ks], b[ks], d0); d1 = MFMA(a1[ks], b[ks], d1); }
  const float sc = mat ? 1.0f : 0.07216878364870322f * 1.4426950408889634f;
  u32x4 o;
  o[0] = pack2(d0[0] * sc, d0[1] * sc); o[1] = pack2(d0[2] * sc, d0[3] * sc);
  o[2] = pack2(d1[0] * sc, d1[1] * sc); o[3] = pack2(d1[2] * sc, d1[3] * sc);
  wf[f * 64 + lane] = o;
}

DI void glds16(const char* sbase, unsigned voff, unsigned lds_dst) {
  unsigned keep;
  asm volatile("s_mov_b32 %0, m0\n\ts_mov_b32 m0, %3\n\ts_nop 0\n\tglobal_load_lds_dwordx4 %1, %2\n\ts_mov_b32 m0, %0"
               : "=&s"(keep) : "v"(voff), "s"(sbase), "s"(lds_dst) : "memory");
}
template <int OFF> DI void gld16(bf16x8& d, const char* sbase, unsigned voff) {
  asm volatile("global_load_dwordx4 %0, %1, %2 offset:%3" : "=v"(d) : "v"(voff), "s"(sbase), "i"(OFF) : "memory");
}
#define BARRIER() asm volatile("s_waitcnt lgkmcnt(0)\n\ts_barrier" ::: "memory")
#define WAITV(N) asm volatile("s_waitcnt vmcnt(" #N ")" ::: "memory")
template <int N> DI void waitw(bf16x8& a, bf16x8& b, bf16x8& c) {
  asm volatile("s_waitcnt vmcnt(%3)" : "+v"(a), "+v"(b), "+v"(c) : "i"(N) : "memory");
}

DI unsigned x_src_off(unsigned p) {
  const unsigned t = (p * 10923u) >> 18;
  const unsigned pos = p - 24u * t;
  const unsigned c = pos ^ ((t >> 1) & 7u);
  const unsigned tc = t < 195u ? t : 195u;
  const unsigned win = (tc * 1338u) >> 16;
  const unsigned rem = tc - 49u * win;
  const unsigned r = (rem * 37u) >> 8;
  return (tc + 217u * r - 42u * win) * 384u + 16u * c;
}

DI float xrow_max(float x) {
  auto s = __builtin_amdgcn_permlane16_swap(__float_as_uint(x), __float_as_uint(x), false, false);
  x = __builtin_fmaxf(__uint_as_float(s[0]), __uint_as_float(s[1]));
  auto t = __builtin_amdgcn_permlane32_swap(__float_as_uint(x), __float_as_uint(x), false, false);
  return __builtin_fmaxf(__uint_as_float(t[0]), __uint_as_float(t[1]));
}
DI float xrow_sum(float x) {
  auto s = __builtin_amdgcn_permlane16_swap(__float_as_uint(x), __float_as_uint(x), false, false);
  x = __uint_as_float(s[0]) + __uint_as_float(s[1]);
  auto t = __builtin_amdgcn_permlane32_swap(__float_as_uint(x), __float_as_uint(x), false, false);
  return __uint_as_float(t[0]) + __uint_as_float(t[1]);
}

#define LD128(off) (*(const LAS bf16x8*)(lds + (off)))

__global__ __launch_bounds__(512, 2) void attn_kernel(const char* __restrict__ x, const char* __restrict__ wf,
                                                      char* __restrict__ out) {
  __shared__ __attribute__((aligned(1024))) char smem[LDS_BYTES];
  LAS char* lds = (LAS char*)smem;
  const unsigned lds0 = (unsigned)(uintptr_t)lds;
  const int tid = threadIdx.x, lane = tid & 63;
  const int wid = __builtin_amdgcn_readfirstlane(tid >> 6);
  const int w = wid & 3;
  const int idx = lane & 15, g = lane >> 4;
  const int blk = blockIdx.x;
  const size_t gbase = (size_t)(blk >> 3) * (7 * IMG_ROWB) + (size_t)(blk & 7) * (28 * ROWB);
  const char* xb = x + gbase;
  const char* wb = wf + wid * 18432 + 4096;
  const unsigned l16 = lane * 16;
  unsigned xa0 = LDS_X + ROWB * idx + 16 * (g ^ (idx >> 1));
  unsigned xa1 = LDS_X + ROWB * idx + 16 * ((g + 4) ^ (idx >> 1));
#define XF(T, s) LD128(((s) & 1 ? xa1 : xa0) + 6144 * (T) + 128 * ((s) >> 1))
  const f32x4 zero4 = {0.f, 0.f, 0.f, 0.f};
  bf16x8 wt[6][3];

#define LOADW()                                                                                          \
  do {                                                                                                   \
    gld16<-4096>(wt[0][0], wb, l16); gld16<-3072>(wt[0][1], wb, l16); gld16<-2048>(wt[0][2], wb, l16);   \
    gld16<-1024>(wt[1][0], wb, l16); gld16<0>(wt[1][1], wb, l16); gld16<1024>(wt[1][2], wb, l16);        \
    gld16<2048>(wt[2][0], wb, l16); gld16<3072>(wt[2][1], wb, l16);                                      \
    const char* wb2 = wb + 8192;                                                                         \
    gld16<-4096>(wt[2][2], wb2, l16); gld16<-3072>(wt[3][0], wb2, l16); gld16<-2048>(wt[3][1], wb2, l16);\
    gld16<-1024>(wt[3][2], wb2, l16); gld16<0>(wt[4][0], wb2, l16); gld16<1024>(wt[4][1], wb2, l16);     \
    gld16<2048>(wt[4][2], wb2, l16); gld16<3072>(wt[5][0], wb2, l16);                                    \
    const char* wb3 = wb + 16384;                                                                        \
    gld16<-4096>(wt[5][1], wb3, l16); gld16<-3072>(wt[5][2], wb3, l16);                                  \
  } while (0)

  if (wid < 4) {
    LOADW();
#pragma unroll
    for (int k = 0; k < 14; ++k) {
      const int n = 24 + 4 * k + w;
      glds16(xb, x_src_off(64 * n + lane), lds0 + LDS_X + 1024 * n);
    }
    const unsigned yw0 = LDS_Y + w * 1024 + l16;
    const unsigned yw1 = LDS_Y + (4 + (w >> 1)) * 1024 + l16 + 8 * (w & 1);
    const unsigned pw = LDS_P + w * 2048 + l16;
#pragma unroll 1
    for (int i = 0; i < 5; ++i) {
      BARRIER();
      if (i == 4) { BARRIER(); break; }
      bf16x8 xf[4][6];
#pragma unroll
      for (int s = 0; s < 6; ++s)
#pragma unroll
        for (int T = 1; T < 4; ++T) xf[T][s] = XF(T, s);
#pragma unroll
      for (int s = 0; s < 6; ++s) xf[0][s] = XF(0, s);
      f32x4 acc[4][3];
#pragma unroll
      for (int T = 0; T < 4; ++T)
#pragma unroll
        for (int t = 0; t < 3; ++t) acc[T][t] = zero4;
#define YSTEP(s, N)                                                                    \
      waitw<N>(wt[s][0], wt[s][1], wt[s][2]);                                          \
      _Pragma("unroll") for (int T = 1; T < 4; ++T)                                    \
        _Pragma("unroll") for (int t = 0; t < 3; ++t) acc[T][t] = MFMA(wt[s][t], xf[T][s], acc[T][t]);
      YSTEP(0, 29) YSTEP(1, 26) YSTEP(2, 23) YSTEP(3, 20) YSTEP(4, 17) YSTEP(5, 14)
#define YWRITE(T)                                                                                        \
      do {                                                                                               \
        const unsigned so = (unsigned)(((T) - i) & 3) * 6144u;                                           \
        u32x4 v; v[0] = pack2(acc[T][0][0], acc[T][0][1]); v[1] = pack2(acc[T][0][2], acc[T][0][3]);     \
        v[2] = pack2(acc[T][1][0], acc[T][1][1]); v[3] = pack2(acc[T][1][2], acc[T][1][3]);              \
        *(LAS u32x4*)(lds + yw0 + so) = v;                                                               \
        u32x2 h; h[0] = pack2(acc[T][2][0], acc[T][2][1]); h[1] = pack2(acc[T][2][2], acc[T][2][3]);     \
        *(LAS u32x2*)(lds + yw1 + so) = h;                                                               \
      } while (0)
      YWRITE(1); YWRITE(2); YWRITE(3);
      if (i == 0) {
#pragma unroll
        for (int s = 0; s < 6; ++s)
#pragma unroll
          for (int t = 0; t < 3; ++t) acc[0][t] = MFMA(wt[s][t], xf[0][s], acc[0][t]);
        YWRITE(0);
      }
      f32x4 sa[4];
      const float ninf = -__builtin_inff();
#pragma unroll
      for (int r = 0; r < 4; ++r) {
        sa[0][r] = (g == 0 && r < i) ? ninf : 0.f;
        sa[1][r] = 0.f; sa[2][r] = 0.f;
        sa[3][r] = (g == 0 && r <= i) ? 0.f : ninf;
      }
      BARRIER();
      const unsigned yr = LDS_Y + (unsigned)((w - i) & 3) * 6144u + l16;
      bf16x8 yq[6];
#pragma unroll
      for (int u = 0; u < 6; ++u) yq[u] = LD128(yr + 1024 * u);
#pragma unroll
      for (int u = 0; u < 6; ++u)
#pragma unroll
        for (int kt = 0; kt < 4; ++kt) sa[kt] = MFMA(xf[kt][u], yq[u], sa[kt]);
      float m = __builtin_fmaxf(__builtin_fmaxf(sa[0][0], sa[0][1]), __builtin_fmaxf(sa[0][2], sa[0][3]));
#pragma unroll
      for (int kt = 1; kt < 4; ++kt)
        m = __builtin_fmaxf(__builtin_fmaxf(__builtin_fmaxf(m, sa[kt][0]), sa[kt][1]),
                            __builtin_fmaxf(sa[kt][2], sa[kt][3]));
      m = xrow_max(m);
      float sum = 0.f;
#pragma unroll
      for (int kt = 0; kt < 4; ++kt)
#pragma unroll
        for (int r = 0; r < 4; ++r) { sa[kt][r] = __builtin_amdgcn_exp2f(sa[kt][r] - m); sum += sa[kt][r]; }
      sum = xrow_sum(sum);
      const float rs = __builtin_amdgcn_rcpf(sum);
#pragma unroll
      for (int v = 0; v < 2; ++v) {
        u32x4 p;
        p[0] = pack2(sa[2 * v][0] * rs, sa[2 * v][1] * rs); p[1] = pack2(sa[2 * v][2] * rs, sa[2 * v][3] * rs);
        p[2] = pack2(sa[2 * v + 1][0] * rs, sa[2 * v + 1][1] * rs); p[3] = pack2(sa[2 * v + 1][2] * rs, sa[2 * v + 1][3] * rs);
        *(LAS u32x4*)(lds + pw + 1024 * v) = p;
      }
      if (i == 0) WAITV(9); else if (i == 1) WAITV(5); else WAITV(0);
      xa0 += 18432; xa1 += 18432;
    }
  } else {
#pragma unroll
    for (int k = 0; k < 6; ++k) {
      const int n = 4 * k + w;
      glds16(xb, x_src_off(64 * n + lane), lds0 + LDS_X + 1024 * n);
    }
    LOADW();
    unsigned so[5];
#pragma unroll
    for (int n = 0; n < 5; ++n) {
      const unsigned p = 256 * n + 64 * w + lane;
      const unsigned t = (p * 10923u) >> 18, pos = p - 24u * t;
      const unsigned r = (t * 37u) >> 8;
      so[n] = (t + 217u * r) * 384u + 16u * (pos ^ ((t + 3u) & 7u));
    }
    char* ob = out + gbase;
    const unsigned ord = LDS_O + 3 * ROWB + 1024 * w + l16;
    const unsigned pr = LDS_P + l16;
    u32x4 zp[2][3];
    WAITV(18);
#pragma unroll 1
    for (int i = 0; i < 5; ++i) {
      BARRIER();
      if (i > 0) {
        bf16x8 pf[4][2];
#pragma unroll
        for (int qt = 0; qt < 4; ++qt)
#pragma unroll
          for (int v = 0; v < 2; ++v) pf[qt][v] = LD128(pr + 2048 * qt + 1024 * v);
        const int row0 = idx + 4 - i;
        const unsigned sw = (unsigned)row0 & 7u;
        const unsigned ow0 = LDS_O + ROWB * row0 + 16 * ((4 * w + g) ^ sw);
        const unsigned ow1 = LDS_O + ROWB * row0 + 16 * ((16 + 4 * (w >> 1) + g) ^ sw) + 8 * (w & 1);
#pragma unroll
        for (int qt = 0; qt < 4; ++qt) {
          f32x4 oa[3];
#pragma unroll
          for (int t = 0; t < 3; ++t) {
            oa[t] = MFMA(__builtin_bit_cast(bf16x8, zp[0][t]), pf[qt][0], zero4);
            oa[t] = MFMA(__builtin_bit_cast(bf16x8, zp[1][t]), pf[qt][1], oa[t]);
          }
          u32x4 v; v[0] = pack2(oa[0][0], oa[0][1]); v[1] = pack2(oa[0][2], oa[0][3]);
          v[2] = pack2(oa[1][0], oa[1][1]); v[3] = pack2(oa[1][2], oa[1][3]);
          *(LAS u32x4*)(lds + ow0 + 16 * ROWB * qt) = v;
          u32x2 h; h[0] = pack2(oa[2][0], oa[2][1]); h[1] = pack2(oa[2][2], oa[2][3]);
          *(LAS u32x2*)(lds + ow1 + 16 * ROWB * qt) = h;
        }
      } else {
        bf16x8 xf[6];
#pragma unroll
        for (int s = 0; s < 6; ++s) xf[s] = XF(0, s);
        f32x4 za[3] = {zero4, zero4, zero4};
#define ZSTEP0(s, N)                                                                   \
        waitw<N>(wt[s][0], wt[s][1], wt[s][2]);                                        \
        _Pragma("unroll") for (int t = 0; t < 3; ++t) za[t] = MFMA(xf[s], wt[s][t], za[t]);
        ZSTEP0(0, 15) ZSTEP0(1, 12) ZSTEP0(2, 9) ZSTEP0(3, 6) ZSTEP0(4, 3) ZSTEP0(5, 0)
#pragma unroll
        for (int t = 0; t < 3; ++t) { zp[1][t][2] = pack2(za[t][0], za[t][1]); zp[1][t][3] = pack2(za[t][2], za[t][3]); }
      }
      BARRIER();
      if (i > 0) {
#pragma unroll
        for (int n = 0; n < 5; ++n) {
          if (n < 4 || 64 * w + lane < 152) {
            const u32x4 v = *(const LAS u32x4*)(lds + ord + 4096 * n);
            *(u32x4*)(ob + so[n]) = v;
          }
        }
        ob += 7 * ROWB;
      }
      if (i < 4) {
#pragma unroll
        for (int t = 0; t < 3; ++t) { zp[0][t][0] = zp[1][t][2]; zp[0][t][1] = zp[1][t][3]; }
#pragma unroll
        for (int T = 1; T < 4; ++T) {
          bf16x8 xf[6];
#pragma unroll
          for (int s = 0; s < 6; ++s) xf[s] = XF(T, s);
          f32x4 za[3] = {zero4, zero4, zero4};
#pragma unroll
          for (int s = 0; s < 6; ++s)
#pragma unroll
            for (int t = 0; t < 3; ++t) za[t] = MFMA(xf[s], wt[s][t], za[t]);
#pragma unroll
          for (int t = 0; t < 3; ++t) {
            zp[T >> 1][t][2 * (T & 1)] = pack2(za[t][0], za[t][1]);
            zp[T >> 1][t][2 * (T & 1) + 1] = pack2(za[t][2], za[t][3]);
          }
        }
        xa0 += 18432; xa1 += 18432;
      }
    }
  }
}

extern "C" void kernel_launch(void* const* d_in, const int* in_sizes, int n_in, void* d_out, int out_size,
                              void* d_ws, size_t ws_size, hipStream_t stream) {
  const __bf16* x = (const __bf16*)d_in[0];
  const __bf16* wqkv = (const __bf16*)d_in[1];
  const __bf16* wo = (const __bf16*)d_in[2];
  fold_kernel<<<144, 64, 0, stream>>>(wqkv, wo, (u32x4*)d_ws);
  attn_kernel<<<256, 512, 0, stream>>>((const char*)x, (const char*)d_ws, (char*)d_out);
}
```

```cpp
#include <hip/hip_runtime.h>
#include <stdint.h>

typedef __attribute__((ext_vector_type(8))) __bf16 bf16x8;
typedef __attribute__((ext_vector_type(4))) __bf16 bf16x4;
typedef __attribute__((ext_vector_type(4))) short s16x4;
typedef __attribute__((ext_vector_type(4))) float f32x4;
typedef __attribute__((ext_vector_type(4))) unsigned u32x4;
typedef __attribute__((ext_vector_type(2))) unsigned u32x2;
#define LAS __attribute__((address_space(3)))
#define DI __device__ __forceinline__
#define MFMA(a, b, c) __builtin_amdgcn_mfma_f32_16x16x32_bf16((a), (b), (c), 0, 0, 0)
#define SBAR() __builtin_amdgcn_sched_barrier(0)

constexpr int C = 192;
constexpr int ROWB = 384;
constexpr int IMG_ROWB = 224 * ROWB;
constexpr int LDS_X = 0;
constexpr int LDS_Y = 81920;
constexpr int LDS_P = LDS_Y + 24576;
constexpr int LDS_O = LDS_P + 8192;
constexpr int LDS_BYTES = LDS_O + 67 * ROWB;

DI int chmap(int w, int t, int idx) {
  const int g = idx >> 2, r = idx & 3;
  return t < 2 ? 32 * w + 8 * g + 4 * t + r : 128 + 32 * (w >> 1) + 8 * g + 4 * (w & 1) + r;
}

DI unsigned pack2(float a, float b) {
  typedef __attribute__((ext_vector_type(2))) __bf16 bf16x2;
  bf16x2 v; v[0] = (__bf16)a; v[1] = (__bf16)b;
  return __builtin_bit_cast(unsigned, v);
}

__global__ __launch_bounds__(64) void fold_kernel(const __bf16* __restrict__ wqkv, const __bf16* __restrict__ wo,
                                                  u32x4* __restrict__ wf) {
  __shared__ __attribute__((aligned(16))) char tr[192 * 32];
  const int f = blockIdx.x, lane = threadIdx.x;
  const int t = f % 3, s = (f / 3) % 6, w = (f / 18) & 3, mat = f / 72;
  const int idx = lane & 15, g = lane >> 4;
  const int c0 = 32 * s + 8 * (idx >> 2) + (idx & 3);
  const __bf16* ap = wqkv + (mat ? 2 * C : 0) + c0 * (3 * C) + 8 * g;
  bf16x8 a0[6], a1[6], b[6];
#pragma unroll
  for (int ks = 0; ks < 6; ++ks) {
    a0[ks] = *(const bf16x8*)(ap + 32 * ks);
    a1[ks] = *(const bf16x8*)(ap + 4 * 3 * C + 32 * ks);
  }
  if (mat == 0) {
    const __bf16* bp = wqkv + C + chmap(w, t, idx) * (3 * C) + 8 * g;
#pragma unroll
    for (int ks = 0; ks < 6; ++ks) b[ks] = *(const bf16x8*)(bp + 32 * ks);
  } else {
    const __bf16* bp = wo + (lane >> 2) * C + chmap(w, t, 4 * (lane & 3));
    u32x2 v[12];
#pragma unroll
    for (int n = 0; n < 12; ++n) v[n] = *(const u32x2*)(bp + 16 * n * C);
#pragma unroll
    for (int n = 0; n < 12; ++n) *(u32x2*)(tr + 512 * n + 8 * lane) = v[n];
    __syncthreads();
    const LAS char* tp = (const LAS char*)tr + (8 * g + (idx >> 2)) * 32 + 8 * (idx & 3);
#pragma unroll
    for (int ks = 0; ks < 6; ++ks) {
      s16x4 lo = __builtin_amdgcn_ds_read_tr16_b64_v4i16((LAS s16x4*)(tp + 1024 * ks));
      s16x4 hi = __builtin_amdgcn_ds_read_tr16_b64_v4i16((LAS s16x4*)(tp + 1024 * ks + 128));
      b[ks] = __builtin_bit_cast(bf16x8, __builtin_shufflevector(lo, hi, 0, 1, 2, 3, 4, 5, 6, 7));
    }
  }
  f32x4 d0 = {0.f, 0.f, 0.f, 0.f}, d1 = {0.f, 0.f, 0.f, 0.f};
#pragma unroll
  for (int ks = 0; ks < 6; ++ks) { d0 = MFMA(a0[ks], b[ks], d0); d1 = MFMA(a1[ks], b[ks], d1); }
  const float sc = mat ? 1.0f : 0.07216878364870322f * 1.4426950408889634f;
  u32x4 o;
  o[0] = pack2(d0[0] * sc, d0[1] * sc); o[1] = pack2(d0[2] * sc, d0[3] * sc);
  o[2] = pack2(d1[0] * sc, d1[1] * sc); o[3] = pack2(d1[2] * sc, d1[3] * sc);
  wf[f * 64 + lane] = o;
}

DI void glds16(const char* sbase, unsigned voff, unsigned lds_dst) {
  unsigned keep;
  asm volatile("s_mov_b32 %0, m0\n\ts_mov_b32 m0, %3\n\ts_nop 0\n\tglobal_load_lds_dwordx4 %1, %2 nt\n\ts_mov_b32 m0, %0"
               : "=&s"(keep) : "v"(voff), "s"(sbase), "s"(lds_dst) : "memory");
}
template <int OFF> DI void gld16(bf16x8& d, const char* sbase, unsigned voff) {
  asm volatile("global_load_dwordx4 %0, %1, %2 offset:%3" : "=v"(d) : "v"(voff), "s"(sbase), "i"(OFF) : "memory");
}
#define BARRIER() asm volatile("s_waitcnt lgkmcnt(0)\n\ts_barrier" ::: "memory")
#define WAITV(N) asm volatile("s_waitcnt vmcnt(" #N ")" ::: "memory")
template <int N> DI void waitw(bf16x8& a, bf16x8& b, bf16x8& c) {
  asm volatile("s_waitcnt vmcnt(%3)" : "+v"(a), "+v"(b), "+v"(c) : "i"(N) : "memory");
}

DI unsigned x_src_off(unsigned p) {
  const unsigned t = (p * 10923u) >> 18;
  const unsigned pos = p - 24u * t;
  const unsigned c = pos ^ ((t >> 1) & 7u);
  const unsigned tc = t < 195u ? t : 195u;
  const unsigned win = (tc * 1338u) >> 16;
  const unsigned rem = tc - 49u * win;
  const unsigned r = (rem * 37u) >> 8;
  return (tc + 217u * r - 42u * win) * 384u + 16u * c;
}

DI float xrow_max(float x) {
  auto s = __builtin_amdgcn_permlane16_swap(__float_as_uint(x), __float_as_uint(x), false, false);
  x = __builtin_fmaxf(__uint_as_float(s[0]), __uint_as_float(s[1]));
  auto t = __builtin_amdgcn_permlane32_swap(__float_as_uint(x), __float_as_uint(x), false, false);
  return __builtin_fmaxf(__uint_as_float(t[0]), __uint_as_float(t[1]));
}
DI float xrow_sum(float x) {
  auto s = __builtin_amdgcn_permlane16_swap(__float_as_uint(x), __float_as_uint(x), false, false);
  x = __uint_as_float(s[0]) + __uint_as_float(s[1]);
  auto t = __builtin_amdgcn_permlane32_swap(__float_as_uint(x), __float_as_uint(x), false, false);
  return __uint_as_float(t[0]) + __uint_as_float(t[1]);
}

#define LD128(off) (*(const LAS bf16x8*)(lds + (off)))

#define STAMP_LDS 0
#define STAMP(k) do {} while (0)
#define RSTAMP(k) do {} while (0)

__global__ __launch_bounds__(512, 2) void attn_kernel(const char* __restrict__ x, const char* __restrict__ wf,
                                                      char* __restrict__ out) {
  __shared__ __attribute__((aligned(1024))) char smem[LDS_BYTES + STAMP_LDS];
  LAS char* lds = (LAS char*)smem;
  const unsigned lds0 = (unsigned)(uintptr_t)lds;
  const int tid = threadIdx.x, lane = tid & 63;
  const int wid = __builtin_amdgcn_readfirstlane(tid >> 6);
  const int w = wid & 3;
  const int idx = lane & 15, g = lane >> 4;
  const int blk = blockIdx.x;
  const size_t gbase = (size_t)(blk >> 3) * (7 * IMG_ROWB) + (size_t)(blk & 7) * (28 * ROWB);
  const char* xb = x + gbase;
  const char* wb = wf + wid * 18432 + 4096;
  const unsigned l16 = lane * 16;
  unsigned xa0 = LDS_X + ROWB * idx + 16 * (g ^ (idx >> 1));
  unsigned xa1 = LDS_X + ROWB * idx + 16 * ((g + 4) ^ (idx >> 1));
#define XF(T, s) LD128(((s) & 1 ? xa1 : xa0) + 6144 * (T) + 128 * ((s) >> 1))
  const f32x4 zero4 = {0.f, 0.f, 0.f, 0.f};
  bf16x8 wt[6][3];
  RSTAMP(31); STAMP(0);

#define LOADW()                                                                                          \
  do {                                                                                                   \
    gld16<-4096>(wt[0][0], wb, l16); gld16<-3072>(wt[0][1], wb, l16); gld16<-2048>(wt[0][2], wb, l16);   \
    gld16<-1024>(wt[1][0], wb, l16); gld16<0>(wt[1][1], wb, l16); gld16<1024>(wt[1][2], wb, l16);        \
    gld16<2048>(wt[2][0], wb, l16); gld16<3072>(wt[2][1], wb, l16);                                      \
    const char* wb2 = wb + 8192;                                                                         \
    gld16<-4096>(wt[2][2], wb2, l16); gld16<-3072>(wt[3][0], wb2, l16); gld16<-2048>(wt[3][1], wb2, l16);\
    gld16<-1024>(wt[3][2], wb2, l16); gld16<0>(wt[4][0], wb2, l16); gld16<1024>(wt[4][1], wb2, l16);     \
    gld16<2048>(wt[4][2], wb2, l16); gld16<3072>(wt[5][0], wb2, l16);                                    \
    const char* wb3 = wb + 16384;                                                                        \
    gld16<-4096>(wt[5][1], wb3, l16); gld16<-3072>(wt[5][2], wb3, l16);                                  \
  } while (0)

  if (wid < 4) {
    LOADW();
    STAMP(23);
#pragma unroll
    for (int k = 0; k < 14; ++k) {
      const int n = 24 + 4 * k + w;
      glds16(xb, x_src_off(64 * n + lane), lds0 + LDS_X + 1024 * n);
    }
    const unsigned yw0 = LDS_Y + w * 1024 + l16;
    const unsigned yw1 = LDS_Y + (4 + (w >> 1)) * 1024 + l16 + 8 * (w & 1);
    const unsigned pw = LDS_P + w * 2048 + l16;
#pragma unroll 1
    for (int i = 0; i < 5; ++i) {
      STAMP(1 + 4 * i);
      BARRIER();
      STAMP(2 + 4 * i);
      if (i == 4) { BARRIER(); break; }
      bf16x8 xf[4][6];
#pragma unroll
      for (int s = 0; s < 6; ++s)
#pragma unroll
        for (int T = 1; T < 4; ++T) xf[T][s] = XF(T, s);
#pragma unroll
      for (int s = 0; s < 6; ++s) xf[0][s] = XF(0, s);
      f32x4 acc[4][3];
#pragma unroll
      for (int T = 0; T < 4; ++T)
#pragma unroll
        for (int t = 0; t < 3; ++t) acc[T][t] = zero4;
#define YSTEP(s, N)                                                                    \
      waitw<N>(wt[s][0], wt[s][1], wt[s][2]);                                          \
      _Pragma("unroll") for (int T = 1; T < 4; ++T)                                    \
        _Pragma("unroll") for (int t = 0; t < 3; ++t) acc[T][t] = MFMA(wt[s][t], xf[T][s], acc[T][t]);
      YSTEP(0, 29) YSTEP(1, 26) YSTEP(2, 23) YSTEP(3, 20) YSTEP(4, 17) YSTEP(5, 14)
#define YWRITE(T)                                                                                        \
      do {                                                                                               \
        const unsigned so = (unsigned)(((T) - i) & 3) * 6144u;                                           \
        u32x4 v; v[0] = pack2(acc[T][0][0], acc[T][0][1]); v[1] = pack2(acc[T][0][2], acc[T][0][3]);     \
        v[2] = pack2(acc[T][1][0], acc[T][1][1]); v[3] = pack2(acc[T][1][2], acc[T][1][3]);              \
        *(LAS u32x4*)(lds + yw0 + so) = v;                                                               \
        u32x2 h; h[0] = pack2(acc[T][2][0], acc[T][2][1]); h[1] = pack2(acc[T][2][2], acc[T][2][3]);     \
        *(LAS u32x2*)(lds + yw1 + so) = h;                                                               \
      } while (0)
      YWRITE(1); YWRITE(2); YWRITE(3);
      if (i == 0) {
#pragma unroll
        for (int s = 0; s < 6; ++s)
#pragma unroll
          for (int t = 0; t < 3; ++t) acc[0][t] = MFMA(wt[s][t], xf[0][s], acc[0][t]);
        YWRITE(0);
      }
      f32x4 sa[4];
      const float ninf = -__builtin_inff();
#pragma unroll
      for (int r = 0; r < 4; ++r) {
        sa[0][r] = (g == 0 && r < i) ? ninf : 0.f;
        sa[1][r] = 0.f; sa[2][r] = 0.f;
        sa[3][r] = (g == 0 && r <= i) ? 0.f : ninf;
      }
      STAMP(3 + 4 * i);
      BARRIER();
      STAMP(4 + 4 * i);
      const unsigned yr = LDS_Y + (unsigned)((w - i) & 3) * 6144u + l16;
      bf16x8 yq[6];
#pragma unroll
      for (int u = 0; u < 6; ++u) yq[u] = LD128(yr + 1024 * u);
#pragma unroll
      for (int u = 0; u < 6; ++u)
#pragma unroll
        for (int kt = 0; kt < 4; ++kt) sa[kt] = MFMA(xf[kt][u], yq[u], sa[kt]);
      float m = __builtin_fmaxf(__builtin_fmaxf(sa[0][0], sa[0][1]), __builtin_fmaxf(sa[0][2], sa[0][3]));
#pragma unroll
      for (int kt = 1; kt < 4; ++kt)
        m = __builtin_fmaxf(__builtin_fmaxf(__builtin_fmaxf(m, sa[kt][0]), sa[kt][1]),
                            __builtin_fmaxf(sa[kt][2], sa[kt][3]));
      m = xrow_max(m);
      float sum = 0.f;
#pragma unroll
      for (int kt = 0; kt < 4; ++kt)
#pragma unroll
        for (int r = 0; r < 4; ++r) { sa[kt][r] = __builtin_amdgcn_exp2f(sa[kt][r] - m); sum += sa[kt][r]; }
      sum = xrow_sum(sum);
      const float rs = __builtin_amdgcn_rcpf(sum);
#pragma unroll
      for (int v = 0; v < 2; ++v) {
        u32x4 p;
        p[0] = pack2(sa[2 * v][0] * rs, sa[2 * v][1] * rs); p[1] = pack2(sa[2 * v][2] * rs, sa[2 * v][3] * rs);
        p[2] = pack2(sa[2 * v + 1][0] * rs, sa[2 * v + 1][1] * rs); p[3] = pack2(sa[2 * v + 1][2] * rs, sa[2 * v + 1][3] * rs);
        *(LAS u32x4*)(lds + pw + 1024 * v) = p;
      }
      if (i == 0) WAITV(9); else if (i == 1) WAITV(5); else WAITV(0);
      xa0 += 18432; xa1 += 18432;
    }
  } else {
#pragma unroll
    for (int k = 0; k < 6; ++k) {
      const int n = 4 * k + w;
      glds16(xb, x_src_off(64 * n + lane), lds0 + LDS_X + 1024 * n);
    }
    STAMP(23);
    LOADW();
    STAMP(24);
    unsigned so[5];
#pragma unroll
    for (int n = 0; n < 5; ++n) {
      const unsigned p = 256 * n + 64 * w + lane;
      const unsigned t = (p * 10923u) >> 18, pos = p - 24u * t;
      const unsigned r = (t * 37u) >> 8;
      so[n] = (t + 217u * r) * 384u + 16u * (pos ^ ((t + 3u) & 7u));
    }
    char* ob = out + gbase;
    const unsigned ord = LDS_O + 3 * ROWB + 1024 * w + l16;
    const unsigned pr = LDS_P + l16;
    u32x4 zp[2][3];
    WAITV(18);
#pragma unroll 1
    for (int i = 0; i < 5; ++i) {
      STAMP(1 + 4 * i);
      BARRIER();
      STAMP(2 + 4 * i);
      if (i > 0) {
        bf16x8 pf[4][2];
#pragma unroll
        for (int qt = 0; qt < 4; ++qt)
#pragma unroll
          for (int v = 0; v < 2; ++v) pf[qt][v] = LD128(pr + 2048 * qt + 1024 * v);
        const int row0 = idx + 4 - i;
        const unsigned sw = (unsigned)row0 & 7u;
        const unsigned ow0 = LDS_O + ROWB * row0 + 16 * ((4 * w + g) ^ sw);
        const unsigned ow1 = LDS_O + ROWB * row0 + 16 * ((16 + 4 * (w >> 1) + g) ^ sw) + 8 * (w & 1);
#pragma unroll
        for (int qt = 0; qt < 4; ++qt) {
          f32x4 oa[3];
#pragma unroll
          for (int t = 0; t < 3; ++t) {
            oa[t] = MFMA(__builtin_bit_cast(bf16x8, zp[0][t]), pf[qt][0], zero4);
            oa[t] = MFMA(__builtin_bit_cast(bf16x8, zp[1][t]), pf[qt][1], oa[t]);
          }
          u32x4 v; v[0] = pack2(oa[0][0], oa[0][1]); v[1] = pack2(oa[0][2], oa[0][3]);
          v[2] = pack2(oa[1][0], oa[1][1]); v[3] = pack2(oa[1][2], oa[1][3]);
          *(LAS u32x4*)(lds + ow0 + 16 * ROWB * qt) = v;
          u32x2 h; h[0] = pack2(oa[2][0], oa[2][1]); h[1] = pack2(oa[2][2], oa[2][3]);
          *(LAS u32x2*)(lds + ow1 + 16 * ROWB * qt) = h;
        }
      } else {
        bf16x8 xf[6];
#pragma unroll
        for (int s = 0; s < 6; ++s) xf[s] = XF(0, s);
        f32x4 za[3] = {zero4, zero4, zero4};
#define ZSTEP0(s, N)                                                                   \
        waitw<N>(wt[s][0], wt[s][1], wt[s][2]);                                        \
        _Pragma("unroll") for (int t = 0; t < 3; ++t) za[t] = MFMA(xf[s], wt[s][t], za[t]);
        ZSTEP0(0, 15) ZSTEP0(1, 12) ZSTEP0(2, 9) ZSTEP0(3, 6) ZSTEP0(4, 3) ZSTEP0(5, 0)
#pragma unroll
        for (int t = 0; t < 3; ++t) { zp[1][t][2] = pack2(za[t][0], za[t][1]); zp[1][t][3] = pack2(za[t][2], za[t][3]); }
      }
      STAMP(3 + 4 * i);
      BARRIER();
      STAMP(4 + 4 * i);
      if (i > 0) {
#pragma unroll
        for (int n = 0; n < 5; ++n) {
          if (n < 4 || 64 * w + lane < 152) {
            const u32x4 v = *(const LAS u32x4*)(lds + ord + 4096 * n);
            asm volatile("global_store_dwordx4 %0, %1, %2 sc1\n\ts_nop 1" :: "v"(so[n]), "v"(v), "s"(ob) : "memory");
          }
        }
        ob += 7 * ROWB;
      }
      if (i < 4) {
#pragma unroll
        for (int t = 0; t < 3; ++t) { zp[0][t][0] = zp[1][t][2]; zp[0][t][1] = zp[1][t][3]; }
#pragma unroll
        for (int T = 1; T < 4; ++T) {
          bf16x8 xf[6];
#pragma unroll
          for (int s = 0; s < 6; ++s) xf[s] = XF(T, s);
          f32x4 za[3] = {zero4, zero4, zero4};
#pragma unroll
          for (int s = 0; s < 6; ++s)
#pragma unroll
            for (int t = 0; t < 3; ++t) za[t] = MFMA(xf[s], wt[s][t], za[t]);
#pragma unroll
          for (int t = 0; t < 3; ++t) {
            zp[T >> 1][t][2 * (T & 1)] = pack2(za[t][0], za[t][1]);
            zp[T >> 1][t][2 * (T & 1) + 1] = pack2(za[t][2], za[t][3]);
          }
        }
        xa0 += 18432; xa1 += 18432;
      }
    }
  }
}

extern "C" void kernel_launch(void* const* d_in, const int* in_sizes, int n_in, void* d_out, int out_size,
                              void* d_ws, size_t ws_size, hipStream_t stream) {
  const __bf16* x = (const __bf16*)d_in[0];
  const __bf16* wqkv = (const __bf16*)d_in[1];
  const __bf16* wo = (const __bf16*)d_in[2];
  fold_kernel<<<144, 64, 0, stream>>>(wqkv, wo, (u32x4*)d_ws);
  attn_kernel<<<256, 512, 0, stream>>>((const char*)x, (const char*)d_ws, (char*)d_out);
}
```

```cpp
#include <hip/hip_runtime.h>

constexpr int LDS_BYTES = 208 * 384 + 8 * 6144 + 16384 + 8192;

__global__ __launch_bounds__(512, 2) void attn_kernel(const char* x, const char* wqkv, const char* wo, char* wf, char* out) {
  __shared__ __attribute__((aligned(1024))) char smem[LDS_BYTES];
  asm volatile(
    "s_load_dwordx8 s[4:11], s[0:1], 0x0\n"
    "s_load_dwordx2 s[12:13], s[0:1], 0x20\n"
    "s_getpc_b64 s[14:15]\n"
    "Lpc_%=:\n"
    "v_subrev_u32 v1, 448, v0\n"
    "s_bfe_u32 s20, s2, 0x30003\n"
    "s_mul_i32 s20, s20, 64\n"
    "v_cmp_gt_u32 vcc, 64, v1\n"
    "v_add_u32 v1, s20, v1\n"
    "v_lshlrev_b32 v1, 6, v1\n"
    "v_min_u32 v1, Lend_%=-Lpc_%=-4, v1\n"
    "s_cbranch_vccz Lcp_%=\n"
    "global_load_dword v255, v1, s[14:15]\n"
    "Lcp_%=:\n"
    "v_readfirstlane_b32 s3, v0\n"
    "v_and_b32 v32, 63, v0\n"
    "s_memrealtime s[16:17]\n"
    "s_and_b32 s18, s2, 3\n"
    "s_mul_i32 s18, s18, 36\n"
    "s_lshr_b32 s19, s2, 3\n"
    "s_lshr_b32 s3, s3, 6\n"
    "s_lshl_b32 s20, s3, 5\n"
    "s_add_u32 s21, s20, s19\n"
    "s_bfe_u32 s22, s2, 0x10002\n"
    "s_mul_i32 s22, s22, 18\n"
    "s_add_u32 s20, s21, s22\n"
    "s_cmp_ge_u32 s20, 36\n"
    "s_cselect_b32 s22, 36, 0\n"
    "s_sub_u32 s20, s20, s22\n"
    "s_add_u32 s18, s18, s20\n"
    "s_mov_b32 s33, 0\n"
    "s_cmp_lt_u32 s21, 36\n"
    "s_cbranch_scc0 Lnp_%=\n"
    "s_setprio 3\n"
    "s_mul_i32 s34, s3, 0x1800\n"
    "Lfold_%=:\n"
    "s_mul_i32 s22, s18, 171\n"
    "s_lshr_b32 s22, s22, 9\n"
    "s_mul_i32 s23, s22, 3\n"
    "s_sub_u32 s23, s18, s23\n"
    "s_mul_i32 s24, s18, 57\n"
    "s_lshr_b32 s24, s24, 10\n"
    "s_mul_i32 s25, s24, 6\n"
    "s_sub_u32 s25, s22, s25\n"
    "v_bfe_u32 v33, v32, 2, 2\n"
    "v_and_b32 v34, 3, v32\n"
    "v_lshl_or_b32 v35, v33, 3, v34\n"
    "v_mul_u32_u24 v35, 0x480, v35\n"
    "v_and_b32 v36, 48, v32\n"
    "v_add_u32 v35, v35, v36\n"
    "s_mul_i32 s25, s25, 0x9000\n"
    "s_lshr_b32 s26, s24, 2\n"
    "s_mul_i32 s27, s26, 0x300\n"
    "s_add_u32 s25, s25, s27\n"
    "s_and_b32 s24, s24, 3\n"
    "s_lshl_b32 s27, s24, 5\n"
    "s_lshl_b32 s28, s23, 2\n"
    "s_add_u32 s27, s27, s28\n"
    "s_lshr_b32 s28, s24, 1\n"
    "s_lshl_b32 s28, s28, 5\n"
    "s_and_b32 s29, s24, 1\n"
    "s_lshl_b32 s29, s29, 2\n"
    "s_add_u32 s28, s28, s29\n"
    "s_add_u32 s28, s28, 128\n"
    "s_cmp_lt_u32 s23, 2\n"
    "s_cselect_b32 s27, s27, s28\n"
    "s_waitcnt lgkmcnt(0)\n"
    "s_add_u32 s28, s6, s25\n"
    "s_addc_u32 s29, s7, 0\n"
    "s_add_u32 s30, s28, 0x1200\n"
    "s_addc_u32 s31, s29, 0\n"
    "s_cmp_eq_u32 s26, 0\n"
    "s_cbranch_scc1 Lm_%=\n"
    "v_lshrrev_b32 v36, 2, v32\n"
    "v_mul_u32_u24 v37, 0x1200, v36\n"
    "v_lshl_add_u32 v37, v34, 4, v37\n"
    "s_lshl_b32 s27, s27, 1\n"
    "s_add_u32 s27, s27, 0x900\n"
    "s_add_u32 s22, s8, s27\n"
    "s_addc_u32 s23, s9, 0\n"
    "global_load_dwordx2 v[112:113], v37, s[22:23] offset:-2304\n"
    "global_load_dwordx2 v[114:115], v37, s[22:23] offset:-1920\n"
    "global_load_dwordx2 v[116:117], v37, s[22:23] offset:-1536\n"
    "global_load_dwordx2 v[118:119], v37, s[22:23] offset:-1152\n"
    "global_load_dwordx2 v[120:121], v37, s[22:23] offset:-768\n"
    "global_load_dwordx2 v[122:123], v37, s[22:23] offset:-384\n"
    "global_load_dwordx2 v[124:125], v37, s[22:23] offset:0\n"
    "global_load_dwordx2 v[126:127], v37, s[22:23] offset:384\n"
    "global_load_dwordx2 v[128:129], v37, s[22:23] offset:768\n"
    "global_load_dwordx2 v[130:131], v37, s[22:23] offset:1152\n"
    "global_load_dwordx2 v[132:133], v37, s[22:23] offset:1536\n"
    "global_load_dwordx2 v[134:135], v37, s[22:23] offset:1920\n"
    "s_branch La_%=\n"
    "Lm_%=:\n"
    "s_mul_i32 s27, s27, 0x480\n"
    "s_add_u32 s27, s27, 0x180\n"
    "s_add_u32 s22, s6, s27\n"
    "s_addc_u32 s23, s7, 0\n"
    "global_load_dwordx4 v[88:91], v35, s[22:23] offset:0\n"
    "global_load_dwordx4 v[92:95], v35, s[22:23] offset:64\n"
    "global_load_dwordx4 v[96:99], v35, s[22:23] offset:128\n"
    "global_load_dwordx4 v[100:103], v35, s[22:23] offset:192\n"
    "global_load_dwordx4 v[104:107], v35, s[22:23] offset:256\n"
    "global_load_dwordx4 v[108:111], v35, s[22:23] offset:320\n"
    "La_%=:\n"
    "global_load_dwordx4 v[40:43], v35, s[28:29] offset:0\n"
    "global_load_dwordx4 v[64:67], v35, s[30:31] offset:0\n"
    "global_load_dwordx4 v[44:47], v35, s[28:29] offset:64\n"
    "global_load_dwordx4 v[68:71], v35, s[30:31] offset:64\n"
    "global_load_dwordx4 v[48:51], v35, s[28:29] offset:128\n"
    "global_load_dwordx4 v[72:75], v35, s[30:31] offset:128\n"
    "global_load_dwordx4 v[52:55], v35, s[28:29] offset:192\n"
    "global_load_dwordx4 v[76:79], v35, s[30:31] offset:192\n"
    "global_load_dwordx4 v[56:59], v35, s[28:29] offset:256\n"
    "global_load_dwordx4 v[80:83], v35, s[30:31] offset:256\n"
    "global_load_dwordx4 v[60:63], v35, s[28:29] offset:320\n"
    "global_load_dwordx4 v[84:87], v35, s[30:31] offset:320\n"
    "s_lshl_b32 s27, s18, 4\n"
    "s_add_u32 s27, s27, 147456\n"
    "s_add_u32 s68, s10, s27\n"
    "s_addc_u32 s69, s11, 0\n"
    "v_lshlrev_b32 v39, 12, v32\n"
    "v_mov_b32 v38, 1\n"
    "Lpost_%=:\n"
    "s_mov_b32 s27, 0x3dd53b94\n"
    "s_cmp_eq_u32 s26, 0\n"
    "s_cbranch_scc1 Lmm_%=\n"
    "s_mov_b32 s27, 1.0\n"
    "v_mul_u32_u24 v37, 0x180, v36\n"
    "v_lshl_add_u32 v37, v34, 3, v37\n"
    "s_add_u32 s22, s34, 79872\n"
    "v_add_u32 v37, s22, v37\n"
    "v_lshrrev_b32 v36, 4, v32\n"
    "v_lshl_or_b32 v36, v36, 3, v33\n"
    "v_lshlrev_b32 v36, 5, v36\n"
    "v_lshl_add_u32 v36, v34, 3, v36\n"
    "v_add_u32 v36, s22, v36\n"
    "s_waitcnt vmcnt(22)\n"
    "ds_write2_b64 v37, v[112:113], v[114:115] offset0:0 offset1:4\n"
    "s_waitcnt vmcnt(20)\n"
    "ds_write2_b64 v37, v[116:117], v[118:119] offset0:8 offset1:12\n"
    "s_waitcnt vmcnt(18)\n"
    "ds_write2_b64 v37, v[120:121], v[122:123] offset0:16 offset1:20\n"
    "s_waitcnt vmcnt(16)\n"
    "ds_write2_b64 v37, v[124:125], v[126:127] offset0:24 offset1:28\n"
    "s_waitcnt vmcnt(14)\n"
    "ds_write2_b64 v37, v[128:129], v[130:131] offset0:32 offset1:36\n"
    "s_waitcnt vmcnt(12)\n"
    "ds_write2_b64 v37, v[132:133], v[134:135] offset0:40 offset1:44\n"
    "s_waitcnt lgkmcnt(0)\n"
    "ds_read_b64_tr_b16 v[88:89], v36 offset:0\n"
    "ds_read_b64_tr_b16 v[90:91], v36 offset:128\n"
    "ds_read_b64_tr_b16 v[92:93], v36 offset:1024\n"
    "ds_read_b64_tr_b16 v[94:95], v36 offset:1152\n"
    "ds_read_b64_tr_b16 v[96:97], v36 offset:2048\n"
    "ds_read_b64_tr_b16 v[98:99], v36 offset:2176\n"
    "ds_read_b64_tr_b16 v[100:101], v36 offset:3072\n"
    "ds_read_b64_tr_b16 v[102:103], v36 offset:3200\n"
    "ds_read_b64_tr_b16 v[104:105], v36 offset:4096\n"
    "ds_read_b64_tr_b16 v[106:107], v36 offset:4224\n"
    "ds_read_b64_tr_b16 v[108:109], v36 offset:5120\n"
    "ds_read_b64_tr_b16 v[110:111], v36 offset:5248\n"
    "Lmm_%=:\n"
    "v_lshlrev_b32 v37, 4, v32\n"
    "s_lshl_b32 s22, s18, 10\n"
    "s_add_u32 s22, s10, s22\n"
    "s_addc_u32 s23, s11, 0\n"
    "s_waitcnt vmcnt(10) lgkmcnt(10)\n"
    "v_mfma_f32_16x16x32_bf16 v[136:139], v[40:43], v[88:91], 0\n"
    "v_mfma_f32_16x16x32_bf16 v[140:143], v[64:67], v[88:91], 0\n"
    "s_waitcnt vmcnt(8) lgkmcnt(8)\n"
    "v_mfma_f32_16x16x32_bf16 v[136:139], v[44:47], v[92:95], v[136:139]\n"
    "v_mfma_f32_16x16x32_bf16 v[140:143], v[68:71], v[92:95], v[140:143]\n"
    "s_waitcnt vmcnt(6) lgkmcnt(6)\n"
    "v_mfma_f32_16x16x32_bf16 v[136:139], v[48:51], v[96:99], v[136:139]\n"
    "v_mfma_f32_16x16x32_bf16 v[140:143], v[72:75], v[96:99], v[140:143]\n"
    "s_waitcnt vmcnt(4) lgkmcnt(4)\n"
    "v_mfma_f32_16x16x32_bf16 v[136:139], v[52:55], v[100:103], v[136:139]\n"
    "v_mfma_f32_16x16x32_bf16 v[140:143], v[76:79], v[100:103], v[140:143]\n"
    "s_waitcnt vmcnt(2) lgkmcnt(2)\n"
    "v_mfma_f32_16x16x32_bf16 v[136:139], v[56:59], v[104:107], v[136:139]\n"
    "v_mfma_f32_16x16x32_bf16 v[140:143], v[80:83], v[104:107], v[140:143]\n"
    "s_waitcnt vmcnt(0) lgkmcnt(0)\n"
    "v_mfma_f32_16x16x32_bf16 v[136:139], v[60:63], v[108:111], v[136:139]\n"
    "v_mfma_f32_16x16x32_bf16 v[140:143], v[84:87], v[108:111], v[140:143]\n"
    "s_nop 7\n"
    "v_mul_f32 v136, s27, v136\n"
    "v_mul_f32 v137, s27, v137\n"
    "v_mul_f32 v138, s27, v138\n"
    "v_mul_f32 v139, s27, v139\n"
    "v_mul_f32 v140, s27, v140\n"
    "v_mul_f32 v141, s27, v141\n"
    "v_mul_f32 v142, s27, v142\n"
    "v_mul_f32 v143, s27, v143\n"
    "v_cvt_pk_bf16_f32 v136, v136, v137\n"
    "v_cvt_pk_bf16_f32 v137, v138, v139\n"
    "v_cvt_pk_bf16_f32 v138, v140, v141\n"
    "v_cvt_pk_bf16_f32 v139, v142, v143\n"
    "global_store_dwordx4 v37, v[136:139], s[22:23] sc1\n"
    "s_cmp_lg_u32 s33, 0\n"
    "s_cbranch_scc1 Lfbn_%=\n"
    "s_barrier\n"
    "s_waitcnt vmcnt(0)\n"
    "s_mov_b32 exec_hi, 0\n"
    "s_mov_b32 exec_lo, 0xff\n"
    "global_atomic_add v39, v38, s[68:69] sc1\n"
    "s_mov_b64 exec, -1\n"
    "s_branch Lasetup_%=\n"
    "Lnp_%=:\n"
    "s_cmp_lt_u32 s3, 4\n"
    "s_cbranch_scc1 Lanp_%=\n"
    "s_and_b32 s49, s3, 3\n"
    "s_and_b32 s23, s2, 7\n"
    "s_lshl_b32 s22, s23, 2\n"
    "s_lshr_b32 s24, s2, 6\n"
    "s_add_u32 s22, s22, s24\n"
    "s_mul_i32 s22, s22, 0x93000\n"
    "s_and_b32 s24, s19, 7\n"
    "s_mul_i32 s24, s24, 0x2a00\n"
    "s_add_u32 s22, s22, s24\n"
    "s_lshl_b32 s48, s49, 10\n"
    "v_mov_b32 v62, -42\n"
    "s_lshl_b32 s24, s49, 6\n"
    "v_add_u32 v59, s24, v32\n"
    "v_mul_u32_u24 v60, 0x2aab, v59\n"
    "v_lshrrev_b32 v60, 18, v60\n"
    "v_lshlrev_b32 v50, 4, v59\n"
    "v_mul_u32_u24 v61, 0x180, v60\n"
    "v_sub_u32 v50, v50, v61\n"
    "v_mul_u32_u24 v61, 37, v60\n"
    "v_lshrrev_b32 v61, 8, v61\n"
    "v_mul_u32_u24 v61, 0xd9, v61\n"
    "v_cmp_gt_u32 vcc, 49, v60\n"
    "v_lshlrev_b32 v40, 3, v60\n"
    "s_nop 0\n"
    "v_cndmask_b32 v61, v62, v61, vcc\n"
    "v_add_u32 v61, v61, v60\n"
    "v_mul_u32_u24 v45, 0x180, v61\n"
    "v_add_u32 v60, 0, v40\n"
    "v_and_b32 v60, 0x70, v60\n"
    "v_xad_u32 v64, v60, v50, v45\n"
    "s_waitcnt lgkmcnt(0)\n"
    "s_add_u32 s46, s4, s22\n"
    "s_addc_u32 s47, s5, 0\n"
    "s_lshl_b32 s24, s23, 12\n"
    "s_add_u32 s24, s24, 147456\n"
    "s_add_u32 s36, s10, s24\n"
    "s_addc_u32 s37, s11, 0\n"
    "v_lshlrev_b32 v2, 4, v32\n"
    "v_add_u32 v171, 0x800, v2\n"
    "v_cmp_gt_u32 vcc, 16, v32\n"
    "s_cmp_lg_u32 s3, 7\n"
    "s_cbranch_scc1 Lns_%=\n"
    "v_cndmask_b32 v171, v2, v171, vcc\n"
    "s_mov_b64 s[38:39], s[36:37]\n"
    "global_load_dword v144, v2, s[38:39] sc1\n"
    "global_load_dword v145, v2, s[38:39] offset:1024 sc1\n"
    "global_load_dword v146, v171, s[38:39] sc1\n"
    "s_add_u32 s38, s38, 0x8000\n"
    "s_addc_u32 s39, s39, 0\n"
    "global_load_dword v147, v2, s[38:39] sc1\n"
    "global_load_dword v148, v2, s[38:39] offset:1024 sc1\n"
    "global_load_dword v149, v171, s[38:39] sc1\n"
    "Lns_%=:\n"
    "s_add_u32 m0, s48, 0\n"
    "s_nop 0\n"
    "global_load_lds_dwordx4 v64, s[46:47] sc1 nt\n"
    "v_add_u32 v59, 0x100, v59\n"
    "v_mul_u32_u24 v60, 0x2aab, v59\n"
    "v_lshrrev_b32 v60, 18, v60\n"
    "v_lshlrev_b32 v51, 4, v59\n"
    "v_mul_u32_u24 v61, 0x180, v60\n"
    "v_sub_u32 v51, v51, v61\n"
    "v_mul_u32_u24 v61, 37, v60\n"
    "v_lshrrev_b32 v61, 8, v61\n"
    "v_mul_u32_u24 v61, 0xd9, v61\n"
    "v_cmp_gt_u32 vcc, 49, v60\n"
    "v_lshlrev_b32 v41, 3, v60\n"
    "s_nop 0\n"
    "v_cndmask_b32 v61, v62, v61, vcc\n"
    "v_add_u32 v61, v61, v60\n"
    "v_mul_u32_u24 v46, 0x180, v61\n"
    "v_add_u32 v60, 0, v41\n"
    "v_and_b32 v60, 0x70, v60\n"
    "v_xad_u32 v65, v60, v51, v46\n"
    "s_add_u32 m0, s48, 4096\n"
    "s_nop 0\n"
    "global_load_lds_dwordx4 v65, s[46:47] sc1 nt\n"
    "v_add_u32 v59, 0x100, v59\n"
    "v_mul_u32_u24 v60, 0x2aab, v59\n"
    "v_lshrrev_b32 v60, 18, v60\n"
    "v_lshlrev_b32 v52, 4, v59\n"
    "v_mul_u32_u24 v61, 0x180, v60\n"
    "v_sub_u32 v52, v52, v61\n"
    "v_mul_u32_u24 v61, 37, v60\n"
    "v_lshrrev_b32 v61, 8, v61\n"
    "v_mul_u32_u24 v61, 0xd9, v61\n"
    "v_cmp_gt_u32 vcc, 49, v60\n"
    "v_lshlrev_b32 v42, 3, v60\n"
    "s_nop 0\n"
    "v_cndmask_b32 v61, v62, v61, vcc\n"
    "v_add_u32 v61, v61, v60\n"
    "v_mul_u32_u24 v47, 0x180, v61\n"
    "v_add_u32 v60, 0, v42\n"
    "v_and_b32 v60, 0x70, v60\n"
    "v_xad_u32 v66, v60, v52, v47\n"
    "s_add_u32 m0, s48, 8192\n"
    "s_nop 0\n"
    "global_load_lds_dwordx4 v66, s[46:47] sc1 nt\n"
    "v_add_u32 v59, 0x100, v59\n"
    "v_mul_u32_u24 v60, 0x2aab, v59\n"
    "v_lshrrev_b32 v60, 18, v60\n"
    "v_lshlrev_b32 v53, 4, v59\n"
    "v_mul_u32_u24 v61, 0x180, v60\n"
    "v_sub_u32 v53, v53, v61\n"
    "v_mul_u32_u24 v61, 37, v60\n"
    "v_lshrrev_b32 v61, 8, v61\n"
    "v_mul_u32_u24 v61, 0xd9, v61\n"
    "v_cmp_gt_u32 vcc, 49, v60\n"
    "v_lshlrev_b32 v43, 3, v60\n"
    "s_nop 0\n"
    "v_cndmask_b32 v61, v62, v61, vcc\n"
    "v_add_u32 v61, v61, v60\n"
    "v_mul_u32_u24 v48, 0x180, v61\n"
    "v_add_u32 v60, 0, v43\n"
    "v_and_b32 v60, 0x70, v60\n"
    "v_xad_u32 v67, v60, v53, v48\n"
    "s_add_u32 m0, s48, 12288\n"
    "s_nop 0\n"
    "global_load_lds_dwordx4 v67, s[46:47] sc1 nt\n"
    "v_add_u32 v59, 0x100, v59\n"
    "v_mul_u32_u24 v60, 0x2aab, v59\n"
    "v_lshrrev_b32 v60, 18, v60\n"
    "v_lshlrev_b32 v54, 4, v59\n"
    "v_mul_u32_u24 v61, 0x180, v60\n"
    "v_sub_u32 v54, v54, v61\n"
    "v_mul_u32_u24 v61, 37, v60\n"
    "v_lshrrev_b32 v61, 8, v61\n"
    "v_mul_u32_u24 v61, 0xd9, v61\n"
    "v_cmp_gt_u32 vcc, 49, v60\n"
    "v_lshlrev_b32 v44, 3, v60\n"
    "s_nop 0\n"
    "v_cndmask_b32 v61, v62, v61, vcc\n"
    "v_add_u32 v61, v61, v60\n"
    "v_mul_u32_u24 v49, 0x180, v61\n"
    "v_add_u32 v60, 0, v44\n"
    "v_and_b32 v60, 0x70, v60\n"
    "v_xad_u32 v68, v60, v54, v49\n"
    "s_add_u32 s52, s46, 2688\n"
    "s_addc_u32 s53, s47, 0\n"
    "s_add_u32 s54, s46, 5376\n"
    "s_addc_u32 s55, s47, 0\n"
    "s_add_u32 s56, s46, 8064\n"
    "s_addc_u32 s57, s47, 0\n"
    "v_add_u32 v60, 392, v40\n"
    "v_and_b32 v60, 0x70, v60\n"
    "v_xad_u32 v69, v60, v50, v45\n"
    "v_add_u32 v60, 392, v41\n"
    "v_and_b32 v60, 0x70, v60\n"
    "v_xad_u32 v175, v60, v51, v46\n"
    "s_barrier\n"
    "s_cmp_eq_u32 s49, 2\n"
    "s_cselect_b32 exec_lo, 0xffffff, 0\n"
    "s_cmp_lt_u32 s49, 2\n"
    "s_cselect_b32 exec_lo, -1, exec_lo\n"
    "s_cselect_b32 exec_hi, -1, 0\n"
    "s_add_u32 m0, s48, 16384\n"
    "s_nop 0\n"
    "global_load_lds_dwordx4 v68, s[46:47] sc1 nt\n"
    "s_mov_b64 exec, -1\n"
    "s_add_u32 m0, s48, 18816\n"
    "s_nop 0\n"
    "global_load_lds_dwordx4 v69, s[52:53] sc1 nt\n"
    "s_cmp_lt_u32 s49, 2\n"
    "s_cbranch_scc0 Lt_%=\n"
    "s_add_u32 m0, s48, 22912\n"
    "s_nop 0\n"
    "global_load_lds_dwordx4 v175, s[52:53] sc1 nt\n"
    "Lt_%=:\n"
    "v_and_b32 v33, 15, v32\n"
    "v_lshrrev_b32 v34, 4, v32\n"
    "v_lshlrev_b32 v35, 4, v32\n"
    "v_lshrrev_b32 v36, 1, v33\n"
    "v_mul_u32_u24 v37, 0x180, v33\n"
    "v_xor_b32 v0, v34, v36\n"
    "v_xor_b32 v1, 4, v0\n"
    "v_lshl_add_u32 v0, v0, 4, v37\n"
    "v_lshl_add_u32 v1, v1, 4, v37\n"
    "s_add_u32 s50, s12, s22\n"
    "s_addc_u32 s51, s13, 0\n"
    "s_mul_i32 s24, s3, 0x4800\n"
    "s_add_u32 s24, s24, 0x1000\n"
    "s_add_u32 s40, s10, s24\n"
    "s_addc_u32 s41, s11, 0\n"
    "s_add_u32 s42, s40, 0x2000\n"
    "s_addc_u32 s43, s41, 0\n"
    "s_add_u32 s44, s40, 0x4000\n"
    "s_addc_u32 s45, s41, 0\n"
    "s_cmp_eq_u32 s49, 2\n"
    "s_cselect_b32 s24, 0xffffff, 0\n"
    "s_cmp_lt_u32 s49, 2\n"
    "s_cselect_b32 s24, -1, s24\n"
    "s_cselect_b32 s25, -1, 0\n"
    "s_mov_b64 s[58:59], s[24:25]\n"
    "v_add_u32 v60, 392, v42\n"
    "v_and_b32 v60, 0x70, v60\n"
    "v_xad_u32 v16, v60, v52, v47\n"
    "v_add_u32 v60, 392, v43\n"
    "v_and_b32 v60, 0x70, v60\n"
    "v_xad_u32 v17, v60, v53, v48\n"
    "v_add_u32 v60, 392, v44\n"
    "v_and_b32 v60, 0x70, v60\n"
    "v_xad_u32 v18, v60, v54, v49\n"
    "v_add_u32 v60, 784, v40\n"
    "v_and_b32 v60, 0x70, v60\n"
    "v_xad_u32 v19, v60, v50, v45\n"
    "v_add_u32 v60, 784, v41\n"
    "v_and_b32 v60, 0x70, v60\n"
    "v_xad_u32 v20, v60, v51, v46\n"
    "v_add_u32 v60, 784, v42\n"
    "v_and_b32 v60, 0x70, v60\n"
    "v_xad_u32 v21, v60, v52, v47\n"
    "v_add_u32 v60, 784, v43\n"
    "v_and_b32 v60, 0x70, v60\n"
    "v_xad_u32 v22, v60, v53, v48\n"
    "v_add_u32 v60, 784, v44\n"
    "v_and_b32 v60, 0x70, v60\n"
    "v_xad_u32 v23, v60, v54, v49\n"
    "v_add_u32 v60, 1176, v40\n"
    "v_and_b32 v60, 0x70, v60\n"
    "v_xad_u32 v24, v60, v50, v45\n"
    "v_add_u32 v60, 1176, v41\n"
    "v_and_b32 v60, 0x70, v60\n"
    "v_xad_u32 v25, v60, v51, v46\n"
    "v_add_u32 v60, 1176, v42\n"
    "v_and_b32 v60, 0x70, v60\n"
    "v_xad_u32 v26, v60, v52, v47\n"
    "v_add_u32 v60, 1176, v43\n"
    "v_and_b32 v60, 0x70, v60\n"
    "v_xad_u32 v27, v60, v53, v48\n"
    "v_add_u32 v60, 1176, v44\n"
    "v_and_b32 v60, 0x70, v60\n"
    "v_xad_u32 v28, v60, v54, v49\n"
    "v_lshlrev_b32 v60, 1, v40\n"
    "v_and_b32 v60, 0x70, v60\n"
    "v_xad_u32 v5, v60, v50, v45\n"
    "v_lshlrev_b32 v60, 1, v41\n"
    "v_and_b32 v60, 0x70, v60\n"
    "v_xad_u32 v6, v60, v51, v46\n"
    "v_lshlrev_b32 v60, 1, v42\n"
    "v_and_b32 v60, 0x70, v60\n"
    "v_xad_u32 v7, v60, v52, v47\n"
    "v_lshlrev_b32 v60, 1, v43\n"
    "v_and_b32 v60, 0x70, v60\n"
    "v_xad_u32 v8, v60, v53, v48\n"
    "v_lshlrev_b32 v60, 1, v44\n"
    "v_and_b32 v60, 0x70, v60\n"
    "v_xad_u32 v9, v60, v54, v49\n"
    "v_add_u32 v3, s48, v35\n"
    "v_add_u32 v4, 129024, v35\n"
    "v_mul_u32_u24 v10, 0x180, v33\n"
    "v_lshlrev_b32 v11, 4, v33\n"
    "v_lshlrev_b32 v60, 4, v34\n"
    "s_lshl_b32 s24, s49, 6\n"
    "v_add_u32 v12, s24, v60\n"
    "s_lshr_b32 s24, s49, 1\n"
    "s_lshl_b32 s24, s24, 6\n"
    "s_and_b32 s25, s49, 1\n"
    "s_lshl_b32 s25, s25, 3\n"
    "s_add_u32 s24, s24, s25\n"
    "s_add_u32 s24, s24, 0x100\n"
    "v_add_u32 v13, s24, v60\n"
    "v_add_u32 v14, 0xf000, v0\n"
    "v_add_u32 v15, 0xf000, v1\n"
    "v_mov_b32 v30, 0\n"
    "v_mov_b32 v31, 0\n"
    "s_lshl_b32 s24, s49, 11\n"
    "s_add_u32 s24, s24, 145408\n"
    "v_lshlrev_b32 v60, 5, v32\n"
    "v_add_u32 v60, s24, v60\n"
    "v_mov_b32 v64, v5\n"
    "v_mov_b32 v65, v6\n"
    "v_mov_b32 v66, v7\n"
    "v_mov_b32 v67, v8\n"
    "v_mov_b32 v68, v9\n"
    "v_mov_b32 v69, v3\n"
    "ds_write_b128 v60, v[64:67]\n"
    "ds_write_b64 v60, v[68:69] offset:16\n"
    "s_mov_b64 s[66:67], s[50:51]\n"
    "s_lshl_b32 s24, s49, 5\n"
    "s_add_u32 s24, s24, s19\n"
    "s_and_b32 s25, s2, 3\n"
    "s_mul_i32 s25, s25, 36\n"
    "s_bfe_u32 s27, s2, 0x10002\n"
    "s_mul_i32 s27, s27, 18\n"
    "s_add_u32 s26, s24, s27\n"
    "s_cmp_ge_u32 s26, 36\n"
    "s_cselect_b32 s27, 36, 0\n"
    "s_sub_u32 s26, s26, s27\n"
    "s_add_u32 s25, s25, s26\n"
    "s_cmp_lt_u32 s24, 36\n"
    "s_cselect_b32 s70, 0xff, 0\n"
    "s_cselect_b32 s25, s25, 0\n"
    "s_mov_b32 s71, 0\n"
    "s_lshl_b32 s25, s25, 4\n"
    "s_add_u32 s25, s25, 180224\n"
    "s_add_u32 s72, s10, s25\n"
    "s_addc_u32 s73, s11, 0\n"
    "s_cmp_lg_u32 s3, 7\n"
    "s_cbranch_scc1 Lbd_%=\n"
    "v_mov_b32 v168, 0\n"
    "v_mov_b32 v169, 0\n"
    "v_mov_b32 v170, 0\n"
    "s_waitcnt vmcnt(0)\n"
    "s_memrealtime s[24:25]\n"
    "s_waitcnt lgkmcnt(0)\n"
    "s_sub_u32 s35, s24, s16\n"
    "s_add_u32 s35, s35, 30\n"
    "s_max_u32 s35, s35, 245\n"
    "s_cmp_lt_u32 s49, 2\n"
    "s_cbranch_scc1 Lua_%=\n"
    "s_add_u32 m0, s48, 22912\n"
    "s_nop 0\n"
    "global_load_lds_dwordx4 v175, s[52:53] sc1 nt\n"
    "Lua_%=:\n"
    "s_add_u32 m0, s48, 27008\n"
    "s_nop 0\n"
    "global_load_lds_dwordx4 v16, s[52:53] sc1 nt\n"
    "s_add_u32 m0, s48, 31104\n"
    "s_nop 0\n"
    "global_load_lds_dwordx4 v17, s[52:53] sc1 nt\n"
    "s_add_u32 m0, s48, 35200\n"
    "s_nop 0\n"
    "global_load_lds_dwordx4 v18, s[52:53] sc1 nt\n"
    "Lw_%=:\n"
    "s_sleep 1\n"
    "s_memrealtime s[24:25]\n"
    "s_waitcnt lgkmcnt(0)\n"
    "s_sub_u32 s24, s24, s16\n"
    "s_cmp_lt_u32 s24, s35\n"
    "s_cbranch_scc1 Lw_%=\n"
    "global_load_dword v156, v2, s[36:37] sc1\n"
    "global_load_dword v157, v2, s[36:37] offset:1024 sc1\n"
    "global_load_dword v158, v171, s[36:37] sc1\n"
    "s_waitcnt vmcnt(0)\n"
    "v_xor_b32 v156, v156, v144\n"
    "v_or_b32 v168, v168, v156\n"
    "v_xor_b32 v157, v157, v145\n"
    "v_or_b32 v169, v169, v157\n"
    "v_xor_b32 v158, v158, v146\n"
    "v_or_b32 v170, v170, v158\n"
    "v_min3_u32 v156, v168, v169, v170\n"
    "v_cmp_eq_u32 vcc, 0, v156\n"
    "s_sub_u32 s24, s24, s16\n"
    "s_nop 1\n"
    "s_cbranch_vccz Lpd_%=\n"
    "Lp_%=:\n"
    "global_load_dword v156, v2, s[36:37] sc1\n"
    "global_load_dword v157, v2, s[36:37] offset:1024 sc1\n"
    "global_load_dword v158, v171, s[36:37] sc1\n"
    "global_load_dword v159, v2, s[38:39] sc1\n"
    "global_load_dword v160, v2, s[38:39] offset:1024 sc1\n"
    "global_load_dword v161, v171, s[38:39] sc1\n"
    "s_memrealtime s[24:25]\n"
    "s_waitcnt vmcnt(0) lgkmcnt(0)\n"
    "v_xor_b32 v156, v156, v144\n"
    "v_or_b32 v168, v168, v156\n"
    "v_xor_b32 v157, v157, v145\n"
    "v_or_b32 v169, v169, v157\n"
    "v_xor_b32 v158, v158, v146\n"
    "v_or_b32 v170, v170, v158\n"
    "v_xor_b32 v159, v159, v147\n"
    "v_or_b32 v168, v168, v159\n"
    "v_xor_b32 v160, v160, v148\n"
    "v_or_b32 v169, v169, v160\n"
    "v_xor_b32 v161, v161, v149\n"
    "v_or_b32 v170, v170, v161\n"
    "v_min3_u32 v156, v168, v169, v170\n"
    "v_cmp_eq_u32 vcc, 0, v156\n"
    "s_sub_u32 s24, s24, s16\n"
    "s_nop 1\n"
    "s_cbranch_vccz Lpd_%=\n"
    "s_sleep 4\n"
    "s_cmp_lt_u32 s24, 5000\n"
    "s_cbranch_scc1 Lp_%=\n"
    "s_mov_b32 s33, 1\n"
    "s_mov_b32 s18, 0\n"
    "s_movk_i32 s34, 0x3000\n"
    "s_branch Lfold_%=\n"
    "Lfbn_%=:\n"
    "s_waitcnt vmcnt(0) lgkmcnt(0)\n"
    "s_add_u32 s18, s18, 1\n"
    "s_cmp_lt_u32 s18, 144\n"
    "s_cbranch_scc1 Lfold_%=\n"
    "Lpd_%=:\n"
    "s_branch Lbgo_%=\n"
    "Lbd_%=:\n"
    "s_waitcnt vmcnt(0)\n"
    "s_cmp_lt_u32 s49, 2\n"
    "s_cbranch_scc1 Lub_%=\n"
    "s_add_u32 m0, s48, 22912\n"
    "s_nop 0\n"
    "global_load_lds_dwordx4 v175, s[52:53] sc1 nt\n"
    "Lub_%=:\n"
    "s_add_u32 m0, s48, 27008\n"
    "s_nop 0\n"
    "global_load_lds_dwordx4 v16, s[52:53] sc1 nt\n"
    "s_add_u32 m0, s48, 31104\n"
    "s_nop 0\n"
    "global_load_lds_dwordx4 v17, s[52:53] sc1 nt\n"
    "s_add_u32 m0, s48, 35200\n"
    "s_nop 0\n"
    "global_load_lds_dwordx4 v18, s[52:53] sc1 nt\n"
    "Lbgo_%=:\n"
    "s_waitcnt lgkmcnt(0)\n"
    "s_barrier\n"
    "ds_read_b128 v[176:179], v0 offset:0\n"
    "ds_read_b128 v[180:183], v0 offset:6144\n"
    "ds_read_b128 v[184:187], v0 offset:12288\n"
    "ds_read_b128 v[188:191], v0 offset:18432\n"
    "ds_read_b128 v[192:195], v1 offset:0\n"
    "ds_read_b128 v[196:199], v1 offset:6144\n"
    "ds_read_b128 v[200:203], v1 offset:12288\n"
    "ds_read_b128 v[204:207], v1 offset:18432\n"
    "s_barrier\n"
    "v_lshlrev_b32 v30, 8, v2\n"
    "s_mov_b64 exec, s[70:71]\n"
    "global_atomic_add v30, v4, s[72:73] sc1\n"
    "s_mov_b64 exec, -1\n"
    "s_add_u32 m0, s48, 37632\n"
    "s_nop 0\n"
    "global_load_lds_dwordx4 v19, s[54:55] sc1 nt\n"
    "s_add_u32 m0, s48, 41728\n"
    "s_nop 0\n"
    "global_load_lds_dwordx4 v20, s[54:55] sc1 nt\n"
    "global_load_dwordx4 v[32:35], v2, s[40:41] offset:-4096 sc1\n"
    "global_load_dwordx4 v[36:39], v2, s[40:41] offset:-3072 sc1\n"
    "global_load_dwordx4 v[40:43], v2, s[40:41] offset:-2048 sc1\n"
    "global_load_dwordx4 v[44:47], v2, s[40:41] offset:-1024 sc1\n"
    "global_load_dwordx4 v[48:51], v2, s[40:41] offset:0 sc1\n"
    "global_load_dwordx4 v[52:55], v2, s[40:41] offset:1024 sc1\n"
    "global_load_dwordx4 v[56:59], v2, s[40:41] offset:2048 sc1\n"
    "global_load_dwordx4 v[60:63], v2, s[40:41] offset:3072 sc1\n"
    "global_load_dwordx4 v[64:67], v2, s[42:43] offset:-4096 sc1\n"
    "global_load_dwordx4 v[68:71], v2, s[42:43] offset:-3072 sc1\n"
    "s_barrier\n"
    "global_load_dwordx4 v[72:75], v2, s[42:43] offset:-2048 sc1\n"
    "global_load_dwordx4 v[76:79], v2, s[42:43] offset:-1024 sc1\n"
    "global_load_dwordx4 v[80:83], v2, s[42:43] offset:0 sc1\n"
    "s_waitcnt vmcnt(10)\n"
    "s_waitcnt lgkmcnt(7)\n"
    "v_mfma_f32_16x16x32_bf16 v[152:155], v[176:179], v[32:35], 0\n"
    "v_mfma_f32_16x16x32_bf16 v[156:159], v[176:179], v[36:39], 0\n"
    "v_mfma_f32_16x16x32_bf16 v[160:163], v[176:179], v[40:43], 0\n"
    "ds_read_b128 v[176:179], v0 offset:128\n"
    "global_load_dwordx4 v[84:87], v2, s[42:43] offset:1024 sc1\n"
    "s_waitcnt lgkmcnt(7)\n"
    "v_mfma_f32_16x16x32_bf16 v[164:167], v[180:183], v[32:35], 0\n"
    "v_mfma_f32_16x16x32_bf16 v[168:171], v[180:183], v[36:39], 0\n"
    "v_mfma_f32_16x16x32_bf16 v[172:175], v[180:183], v[40:43], 0\n"
    "ds_read_b128 v[180:183], v0 offset:6272\n"
    "global_load_dwordx4 v[88:91], v2, s[42:43] offset:2048 sc1\n"
    "s_waitcnt lgkmcnt(2)\n"
    "v_mfma_f32_16x16x32_bf16 v[208:211], v[184:187], v[32:35], 0\n"
    "v_mfma_f32_16x16x32_bf16 v[212:215], v[184:187], v[36:39], 0\n"
    "v_mfma_f32_16x16x32_bf16 v[216:219], v[184:187], v[40:43], 0\n"
    "ds_read_b128 v[184:187], v0 offset:12416\n"
    "s_add_u32 m0, s48, 45824\n"
    "s_nop 0\n"
    "global_load_lds_dwordx4 v21, s[54:55] sc1 nt\n"
    "v_mfma_f32_16x16x32_bf16 v[220:223], v[188:191], v[32:35], 0\n"
    "v_mfma_f32_16x16x32_bf16 v[224:227], v[188:191], v[36:39], 0\n"
    "v_mfma_f32_16x16x32_bf16 v[228:231], v[188:191], v[40:43], 0\n"
    "ds_read_b128 v[188:191], v0 offset:18560\n"
    "s_add_u32 m0, s48, 49920\n"
    "s_nop 0\n"
    "global_load_lds_dwordx4 v22, s[54:55] sc1 nt\n"
    "s_waitcnt vmcnt(11)\n"
    "s_barrier\n"
    "v_mfma_f32_16x16x32_bf16 v[152:155], v[192:195], v[44:47], v[152:155]\n"
    "v_mfma_f32_16x16x32_bf16 v[156:159], v[192:195], v[48:51], v[156:159]\n"
    "v_mfma_f32_16x16x32_bf16 v[160:163], v[192:195], v[52:55], v[160:163]\n"
    "ds_read_b128 v[192:195], v1 offset:128\n"
    "s_add_u32 m0, s48, 54016\n"
    "s_nop 0\n"
    "global_load_lds_dwordx4 v23, s[54:55] sc1 nt\n"
    "v_mfma_f32_16x16x32_bf16 v[164:167], v[196:199], v[44:47], v[164:167]\n"
    "v_mfma_f32_16x16x32_bf16 v[168:171], v[196:199], v[48:51], v[168:171]\n"
    "v_mfma_f32_16x16x32_bf16 v[172:175], v[196:199], v[52:55], v[172:175]\n"
    "ds_read_b128 v[196:199], v1 offset:6272\n"
    "s_add_u32 m0, s48, 56448\n"
    "s_nop 0\n"
    "global_load_lds_dwordx4 v24, s[56:57] sc1 nt\n"
    "v_mfma_f32_16x16x32_bf16 v[208:211], v[200:203], v[44:47], v[208:211]\n"
    "v_mfma_f32_16x16x32_bf16 v[212:215], v[200:203], v[48:51], v[212:215]\n"
    "v_mfma_f32_16x16x32_bf16 v[216:219], v[200:203], v[52:55], v[216:219]\n"
    "ds_read_b128 v[200:203], v1 offset:12416\n"
    "s_add_u32 m0, s48, 60544\n"
    "s_nop 0\n"
    "global_load_lds_dwordx4 v25, s[56:57] sc1 nt\n"
    "v_mfma_f32_16x16x32_bf16 v[220:223], v[204:207], v[44:47], v[220:223]\n"
    "v_mfma_f32_16x16x32_bf16 v[224:227], v[204:207], v[48:51], v[224:227]\n"
    "v_mfma_f32_16x16x32_bf16 v[228:231], v[204:207], v[52:55], v[228:231]\n"
    "ds_read_b128 v[204:207], v1 offset:18560\n"
    "global_load_dwordx4 v[92:95], v2, s[42:43] offset:3072 sc1\n"
    "s_waitcnt vmcnt(12)\n"
    "s_waitcnt lgkmcnt(2)\n"
    "v_mfma_f32_16x16x32_bf16 v[152:155], v[176:179], v[56:59], v[152:155]\n"
    "v_mfma_f32_16x16x32_bf16 v[156:159], v[176:179], v[60:63], v[156:159]\n"
    "v_mfma_f32_16x16x32_bf16 v[160:163], v[176:179], v[64:67], v[160:163]\n"
    "ds_read_b128 v[176:179], v0 offset:256\n"
    "global_load_dwordx4 v[96:99], v2, s[44:45] offset:-4096 sc1\n"
    "v_mfma_f32_16x16x32_bf16 v[164:167], v[180:183], v[56:59], v[164:167]\n"
    "v_mfma_f32_16x16x32_bf16 v[168:171], v[180:183], v[60:63], v[168:171]\n"
    "v_mfma_f32_16x16x32_bf16 v[172:175], v[180:183], v[64:67], v[172:175]\n"
    "ds_read_b128 v[180:183], v0 offset:6400\n"
    "global_load_dwordx4 v[100:103], v2, s[44:45] offset:-3072 sc1\n"
    "v_mfma_f32_16x16x32_bf16 v[208:211], v[184:187], v[56:59], v[208:211]\n"
    "v_mfma_f32_16x16x32_bf16 v[212:215], v[184:187], v[60:63], v[212:215]\n"
    "v_mfma_f32_16x16x32_bf16 v[216:219], v[184:187], v[64:67], v[216:219]\n"
    "ds_read_b128 v[184:187], v0 offset:12544\n"
    "s_add_u32 m0, s48, 64640\n"
    "s_nop 0\n"
    "global_load_lds_dwordx4 v26, s[56:57] sc1 nt\n"
    "v_mfma_f32_16x16x32_bf16 v[220:223], v[188:191], v[56:59], v[220:223]\n"
    "v_mfma_f32_16x16x32_bf16 v[224:227], v[188:191], v[60:63], v[224:227]\n"
    "v_mfma_f32_16x16x32_bf16 v[228:231], v[188:191], v[64:67], v[228:231]\n"
    "ds_read_b128 v[188:191], v0 offset:18688\n"
    "s_add_u32 m0, s48, 68736\n"
    "s_nop 0\n"
    "global_load_lds_dwordx4 v27, s[56:57] sc1 nt\n"
    "s_waitcnt vmcnt(13)\n"
    "v_mfma_f32_16x16x32_bf16 v[152:155], v[192:195], v[68:71], v[152:155]\n"
    "v_mfma_f32_16x16x32_bf16 v[156:159], v[192:195], v[72:75], v[156:159]\n"
    "v_mfma_f32_16x16x32_bf16 v[160:163], v[192:195], v[76:79], v[160:163]\n"
    "ds_read_b128 v[192:195], v1 offset:256\n"
    "s_add_u32 m0, s48, 72832\n"
    "s_nop 0\n"
    "global_load_lds_dwordx4 v28, s[56:57] sc1 nt\n"
    "v_mfma_f32_16x16x32_bf16 v[164:167], v[196:199], v[68:71], v[164:167]\n"
    "v_mfma_f32_16x16x32_bf16 v[168:171], v[196:199], v[72:75], v[168:171]\n"
    "v_mfma_f32_16x16x32_bf16 v[172:175], v[196:199], v[76:79], v[172:175]\n"
    "ds_read_b128 v[196:199], v1 offset:6400\n"
    "s_waitcnt lgkmcnt(2)\n"
    "v_mfma_f32_16x16x32_bf16 v[208:211], v[200:203], v[68:71], v[208:211]\n"
    "v_mfma_f32_16x16x32_bf16 v[212:215], v[200:203], v[72:75], v[212:215]\n"
    "v_mfma_f32_16x16x32_bf16 v[216:219], v[200:203], v[76:79], v[216:219]\n"
    "ds_read_b128 v[200:203], v1 offset:12544\n"
    "v_mfma_f32_16x16x32_bf16 v[220:223], v[204:207], v[68:71], v[220:223]\n"
    "v_mfma_f32_16x16x32_bf16 v[224:227], v[204:207], v[72:75], v[224:227]\n"
    "v_mfma_f32_16x16x32_bf16 v[228:231], v[204:207], v[76:79], v[228:231]\n"
    "ds_read_b128 v[204:207], v1 offset:18688\n"
    "s_waitcnt vmcnt(11)\n"
    "v_mfma_f32_16x16x32_bf16 v[152:155], v[176:179], v[80:83], v[152:155]\n"
    "v_mfma_f32_16x16x32_bf16 v[156:159], v[176:179], v[84:87], v[156:159]\n"
    "v_mfma_f32_16x16x32_bf16 v[160:163], v[176:179], v[88:91], v[160:163]\n"
    "ds_read_b128 v[104:107], v0 offset:24576\n"
    "v_mfma_f32_16x16x32_bf16 v[164:167], v[180:183], v[80:83], v[164:167]\n"
    "v_mfma_f32_16x16x32_bf16 v[168:171], v[180:183], v[84:87], v[168:171]\n"
    "v_mfma_f32_16x16x32_bf16 v[172:175], v[180:183], v[88:91], v[172:175]\n"
    "ds_read_b128 v[108:111], v1 offset:24576\n"
    "v_mfma_f32_16x16x32_bf16 v[208:211], v[184:187], v[80:83], v[208:211]\n"
    "v_mfma_f32_16x16x32_bf16 v[212:215], v[184:187], v[84:87], v[212:215]\n"
    "v_mfma_f32_16x16x32_bf16 v[216:219], v[184:187], v[88:91], v[216:219]\n"
    "ds_read_b128 v[112:115], v0 offset:24704\n"
    "v_mfma_f32_16x16x32_bf16 v[220:223], v[188:191], v[80:83], v[220:223]\n"
    "v_mfma_f32_16x16x32_bf16 v[224:227], v[188:191], v[84:87], v[224:227]\n"
    "v_mfma_f32_16x16x32_bf16 v[228:231], v[188:191], v[88:91], v[228:231]\n"
    "ds_read_b128 v[116:119], v1 offset:24704\n"
    "s_waitcnt vmcnt(3)\n"
    "s_waitcnt lgkmcnt(2)\n"
    "v_mfma_f32_16x16x32_bf16 v[152:155], v[192:195], v[92:95], v[152:155]\n"
    "v_add_u32 v30, 128, v11\n"
    "v_add_u32 v10, 0, v10\n"
    "v_and_b32 v30, 0x70, v30\n"
    "v_mfma_f32_16x16x32_bf16 v[156:159], v[192:195], v[96:99], v[156:159]\n"
    "v_xad_u32 v31, v30, v12, v10\n"
    "v_xad_u32 v16, v30, v13, v10\n"
    "v_mfma_f32_16x16x32_bf16 v[160:163], v[192:195], v[100:103], v[160:163]\n"
    "v_mfma_f32_16x16x32_bf16 v[164:167], v[196:199], v[92:95], v[164:167]\n"
    "v_mfma_f32_16x16x32_bf16 v[168:171], v[196:199], v[96:99], v[168:171]\n"
    "v_mfma_f32_16x16x32_bf16 v[172:175], v[196:199], v[100:103], v[172:175]\n"
    "v_cvt_pk_bf16_f32 v128, v152, v153\n"
    "v_cvt_pk_bf16_f32 v129, v154, v155\n"
    "v_cvt_pk_bf16_f32 v132, v156, v157\n"
    "v_mfma_f32_16x16x32_bf16 v[208:211], v[200:203], v[92:95], v[208:211]\n"
    "v_cvt_pk_bf16_f32 v133, v158, v159\n"
    "v_cvt_pk_bf16_f32 v136, v160, v161\n"
    "v_cvt_pk_bf16_f32 v137, v162, v163\n"
    "v_mfma_f32_16x16x32_bf16 v[212:215], v[200:203], v[96:99], v[212:215]\n"
    "v_mfma_f32_16x16x32_bf16 v[216:219], v[200:203], v[100:103], v[216:219]\n"
    "v_cvt_pk_bf16_f32 v130, v164, v165\n"
    "v_cvt_pk_bf16_f32 v131, v166, v167\n"
    "v_cvt_pk_bf16_f32 v134, v168, v169\n"
    "v_mfma_f32_16x16x32_bf16 v[220:223], v[204:207], v[92:95], v[220:223]\n"
    "v_cvt_pk_bf16_f32 v135, v170, v171\n"
    "v_cvt_pk_bf16_f32 v138, v172, v173\n"
    "v_cvt_pk_bf16_f32 v139, v174, v175\n"
    "v_mfma_f32_16x16x32_bf16 v[224:227], v[204:207], v[96:99], v[224:227]\n"
    "v_mfma_f32_16x16x32_bf16 v[228:231], v[204:207], v[100:103], v[228:231]\n"
    "v_cvt_pk_bf16_f32 v140, v208, v209\n"
    "v_cvt_pk_bf16_f32 v141, v210, v211\n"
    "v_cvt_pk_bf16_f32 v144, v212, v213\n"
    "v_cvt_pk_bf16_f32 v145, v214, v215\n"
    "v_cvt_pk_bf16_f32 v148, v216, v217\n"
    "v_cvt_pk_bf16_f32 v149, v218, v219\n"
    "v_cvt_pk_bf16_f32 v142, v220, v221\n"
    "v_cvt_pk_bf16_f32 v143, v222, v223\n"
    "v_cvt_pk_bf16_f32 v146, v224, v225\n"
    "v_cvt_pk_bf16_f32 v147, v226, v227\n"
    "v_cvt_pk_bf16_f32 v150, v228, v229\n"
    "v_cvt_pk_bf16_f32 v151, v230, v231\n"
    "s_waitcnt lgkmcnt(0)\n"
    "s_barrier\n"
    "ds_read_b128 v[176:179], v4 offset:0\n"
    "ds_read_b128 v[180:183], v4 offset:1024\n"
    "v_mfma_f32_16x16x32_bf16 v[152:155], v[104:107], v[32:35], 0\n"
    "ds_read_b128 v[184:187], v4 offset:2048\n"
    "ds_read_b128 v[188:191], v4 offset:3072\n"
    "v_mfma_f32_16x16x32_bf16 v[156:159], v[104:107], v[36:39], 0\n"
    "ds_read_b128 v[192:195], v4 offset:4096\n"
    "ds_read_b128 v[196:199], v4 offset:5120\n"
    "v_mfma_f32_16x16x32_bf16 v[160:163], v[104:107], v[40:43], 0\n"
    "ds_read_b128 v[200:203], v4 offset:6144\n"
    "ds_read_b128 v[204:207], v4 offset:7168\n"
    "v_mfma_f32_16x16x32_bf16 v[152:155], v[108:111], v[44:47], v[152:155]\n"
    "v_mfma_f32_16x16x32_bf16 v[156:159], v[108:111], v[48:51], v[156:159]\n"
    "v_mfma_f32_16x16x32_bf16 v[160:163], v[108:111], v[52:55], v[160:163]\n"
    "v_mfma_f32_16x16x32_bf16 v[152:155], v[112:115], v[56:59], v[152:155]\n"
    "v_mfma_f32_16x16x32_bf16 v[156:159], v[112:115], v[60:63], v[156:159]\n"
    "v_mfma_f32_16x16x32_bf16 v[160:163], v[112:115], v[64:67], v[160:163]\n"
    "s_waitcnt lgkmcnt(0)\n"
    "v_mfma_f32_16x16x32_bf16 v[208:211], v[128:131], v[176:179], 0\n"
    "s_setprio 2\n"
    "v_mfma_f32_16x16x32_bf16 v[212:215], v[132:135], v[176:179], 0\n"
    "v_mfma_f32_16x16x32_bf16 v[216:219], v[136:139], v[176:179], 0\n"
    "v_mfma_f32_16x16x32_bf16 v[208:211], v[140:143], v[180:183], v[208:211]\n"
    "v_mfma_f32_16x16x32_bf16 v[212:215], v[144:147], v[180:183], v[212:215]\n"
    "v_mfma_f32_16x16x32_bf16 v[216:219], v[148:151], v[180:183], v[216:219]\n"
    "v_mfma_f32_16x16x32_bf16 v[220:223], v[128:131], v[184:187], 0\n"
    "v_mfma_f32_16x16x32_bf16 v[224:227], v[132:135], v[184:187], 0\n"
    "v_mfma_f32_16x16x32_bf16 v[228:231], v[136:139], v[184:187], 0\n"
    "v_mfma_f32_16x16x32_bf16 v[220:223], v[140:143], v[188:191], v[220:223]\n"
    "s_nop 1\n"
    "v_cvt_pk_bf16_f32 v208, v208, v209\n"
    "v_cvt_pk_bf16_f32 v209, v210, v211\n"
    "v_mfma_f32_16x16x32_bf16 v[224:227], v[144:147], v[188:191], v[224:227]\n"
    "v_cvt_pk_bf16_f32 v210, v212, v213\n"
    "v_cvt_pk_bf16_f32 v211, v214, v215\n"
    "v_mfma_f32_16x16x32_bf16 v[228:231], v[148:151], v[188:191], v[228:231]\n"
    "ds_write_b128 v31, v[208:211] offset:0\n"
    "v_mfma_f32_16x16x32_bf16 v[232:235], v[128:131], v[192:195], 0\n"
    "v_cvt_pk_bf16_f32 v216, v216, v217\n"
    "v_cvt_pk_bf16_f32 v217, v218, v219\n"
    "v_mfma_f32_16x16x32_bf16 v[236:239], v[132:135], v[192:195], 0\n"
    "ds_read_b128 v[120:123], v0 offset:24832\n"
    "v_mfma_f32_16x16x32_bf16 v[240:243], v[136:139], v[192:195], 0\n"
    "ds_write_b64 v16, v[216:217] offset:0\n"
    "v_mfma_f32_16x16x32_bf16 v[232:235], v[140:143], v[196:199], v[232:235]\n"
    "v_cvt_pk_bf16_f32 v220, v220, v221\n"
    "v_cvt_pk_bf16_f32 v221, v222, v223\n"
    "v_mfma_f32_16x16x32_bf16 v[236:239], v[144:147], v[196:199], v[236:239]\n"
    "ds_read_b128 v[124:127], v1 offset:24832\n"
    "v_cvt_pk_bf16_f32 v222, v224, v225\n"
    "v_mfma_f32_16x16x32_bf16 v[240:243], v[148:151], v[196:199], v[240:243]\n"
    "v_cvt_pk_bf16_f32 v223, v226, v227\n"
    "v_mfma_f32_16x16x32_bf16 v[208:211], v[128:131], v[200:203], 0\n"
    "ds_write_b128 v31, v[220:223] offset:6144\n"
    "v_mfma_f32_16x16x32_bf16 v[212:215], v[132:135], v[200:203], 0\n"
    "ds_read_b128 v[104:107], v0 offset:30720\n"
    "v_cvt_pk_bf16_f32 v228, v228, v229\n"
    "v_mfma_f32_16x16x32_bf16 v[216:219], v[136:139], v[200:203], 0\n"
    "v_cvt_pk_bf16_f32 v229, v230, v231\n"
    "v_mfma_f32_16x16x32_bf16 v[208:211], v[140:143], v[204:207], v[208:211]\n"
    "ds_write_b64 v16, v[228:229] offset:6144\n"
    "v_mfma_f32_16x16x32_bf16 v[212:215], v[144:147], v[204:207], v[212:215]\n"
    "ds_read_b128 v[108:111], v1 offset:30720\n"
    "v_cvt_pk_bf16_f32 v232, v232, v233\n"
    "v_mfma_f32_16x16x32_bf16 v[216:219], v[148:151], v[204:207], v[216:219]\n"
    "v_cvt_pk_bf16_f32 v233, v234, v235\n"
    "v_cvt_pk_bf16_f32 v234, v236, v237\n"
    "v_mfma_f32_16x16x32_bf16 v[152:155], v[116:119], v[68:71], v[152:155]\n"
    "v_cvt_pk_bf16_f32 v235, v238, v239\n"
    "v_mfma_f32_16x16x32_bf16 v[156:159], v[116:119], v[72:75], v[156:159]\n"
    "ds_read_b128 v[112:115], v0 offset:30848\n"
    "v_mfma_f32_16x16x32_bf16 v[160:163], v[116:119], v[76:79], v[160:163]\n"
    "ds_write_b128 v31, v[232:235] offset:12288\n"
    "s_waitcnt lgkmcnt(4)\n"
    "v_mfma_f32_16x16x32_bf16 v[152:155], v[120:123], v[80:83], v[152:155]\n"
    "v_cvt_pk_bf16_f32 v240, v240, v241\n"
    "v_cvt_pk_bf16_f32 v241, v242, v243\n"
    "v_mfma_f32_16x16x32_bf16 v[156:159], v[120:123], v[84:87], v[156:159]\n"
    "ds_read_b128 v[116:119], v1 offset:30848\n"
    "v_mfma_f32_16x16x32_bf16 v[160:163], v[120:123], v[88:91], v[160:163]\n"
    "ds_write_b64 v16, v[240:241] offset:12288\n"
    "v_mfma_f32_16x16x32_bf16 v[152:155], v[124:127], v[92:95], v[152:155]\n"
    "v_cvt_pk_bf16_f32 v208, v208, v209\n"
    "v_cvt_pk_bf16_f32 v209, v210, v211\n"
    "v_mfma_f32_16x16x32_bf16 v[156:159], v[124:127], v[96:99], v[156:159]\n"
    "ds_read_b128 v[120:123], v0 offset:30976\n"
    "v_cvt_pk_bf16_f32 v210, v212, v213\n"
    "v_mfma_f32_16x16x32_bf16 v[160:163], v[124:127], v[100:103], v[160:163]\n"
    "v_cvt_pk_bf16_f32 v211, v214, v215\n"
    "v_cvt_pk_bf16_f32 v216, v216, v217\n"
    "v_mfma_f32_16x16x32_bf16 v[164:167], v[104:107], v[32:35], 0\n"
    "v_cvt_pk_bf16_f32 v217, v218, v219\n"
    "v_mfma_f32_16x16x32_bf16 v[168:171], v[104:107], v[36:39], 0\n"
    "ds_read_b128 v[124:127], v1 offset:30976\n"
    "v_mfma_f32_16x16x32_bf16 v[172:175], v[104:107], v[40:43], 0\n"
    "s_mov_b32 exec_lo, 0x10001\n"
    "s_mov_b32 exec_hi, 0x10001\n"
    "ds_write_b128 v31, v[208:211] offset:18432\n"
    "ds_write_b64 v16, v[216:217] offset:18432\n"
    "s_mov_b64 exec, -1\n"
    "s_waitcnt lgkmcnt(4)\n"
    "v_mfma_f32_16x16x32_bf16 v[164:167], v[108:111], v[44:47], v[164:167]\n"
    "s_setprio 0\n"
    "s_waitcnt vmcnt(0)\n"
    "v_lshlrev_b32 v30, 8, v2\n"
    "s_mov_b64 exec, s[70:71]\n"
    "global_atomic_add v30, v4, s[72:73] sc1\n"
    "s_mov_b64 exec, -1\n"
    "v_mfma_f32_16x16x32_bf16 v[168:171], v[108:111], v[48:51], v[168:171]\n"
    "ds_read_b128 v[104:107], v0 offset:36864\n"
    "v_cvt_pk_bf16_f32 v128, v152, v153\n"
    "v_mfma_f32_16x16x32_bf16 v[172:175], v[108:111], v[52:55], v[172:175]\n"
    "v_cvt_pk_bf16_f32 v129, v154, v155\n"
    "v_cvt_pk_bf16_f32 v132, v156, v157\n"
    "v_mfma_f32_16x16x32_bf16 v[164:167], v[112:115], v[56:59], v[164:167]\n"
    "v_cvt_pk_bf16_f32 v133, v158, v159\n"
    "v_cvt_pk_bf16_f32 v136, v160, v161\n"
    "v_mfma_f32_16x16x32_bf16 v[168:171], v[112:115], v[60:63], v[168:171]\n"
    "ds_read_b128 v[108:111], v1 offset:36864\n"
    "v_cvt_pk_bf16_f32 v137, v162, v163\n"
    "v_mfma_f32_16x16x32_bf16 v[172:175], v[112:115], v[64:67], v[172:175]\n"
    "v_add_u32 v30, 112, v11\n"
    "v_add_u32 v10, 18432, v10\n"
    "v_mfma_f32_16x16x32_bf16 v[164:167], v[116:119], v[68:71], v[164:167]\n"
    "v_and_b32 v30, 0x70, v30\n"
    "v_xad_u32 v31, v30, v12, v10\n"
    "v_mfma_f32_16x16x32_bf16 v[168:171], v[116:119], v[72:75], v[168:171]\n"
    "ds_read_b128 v[112:115], v0 offset:36992\n"
    "v_xad_u32 v16, v30, v13, v10\n"
    "v_mfma_f32_16x16x32_bf16 v[172:175], v[116:119], v[76:79], v[172:175]\n"
    "s_waitcnt lgkmcnt(2)\n"
    "v_mfma_f32_16x16x32_bf16 v[164:167], v[120:123], v[80:83], v[164:167]\n"
    "v_mfma_f32_16x16x32_bf16 v[168:171], v[120:123], v[84:87], v[168:171]\n"
    "ds_read_b128 v[116:119], v1 offset:36992\n"
    "v_mfma_f32_16x16x32_bf16 v[172:175], v[120:123], v[88:91], v[172:175]\n"
    "v_mfma_f32_16x16x32_bf16 v[164:167], v[124:127], v[92:95], v[164:167]\n"
    "v_mfma_f32_16x16x32_bf16 v[168:171], v[124:127], v[96:99], v[168:171]\n"
    "ds_read_b128 v[120:123], v0 offset:37120\n"
    "v_mfma_f32_16x16x32_bf16 v[172:175], v[124:127], v[100:103], v[172:175]\n"
    "v_mfma_f32_16x16x32_bf16 v[152:155], v[104:107], v[32:35], 0\n"
    "v_mfma_f32_16x16x32_bf16 v[156:159], v[104:107], v[36:39], 0\n"
    "ds_read_b128 v[124:127], v1 offset:37120\n"
    "v_mfma_f32_16x16x32_bf16 v[160:163], v[104:107], v[40:43], 0\n"
    "ds_read_b128 v[104:107], v0 offset:43008\n"
    "s_waitcnt lgkmcnt(3)\n"
    "v_mfma_f32_16x16x32_bf16 v[152:155], v[108:111], v[44:47], v[152:155]\n"
    "v_cvt_pk_bf16_f32 v130, v164, v165\n"
    "v_cvt_pk_bf16_f32 v131, v166, v167\n"
    "v_mfma_f32_16x16x32_bf16 v[156:159], v[108:111], v[48:51], v[156:159]\n"
    "v_cvt_pk_bf16_f32 v134, v168, v169\n"
    "v_cvt_pk_bf16_f32 v135, v170, v171\n"
    "v_mfma_f32_16x16x32_bf16 v[160:163], v[108:111], v[52:55], v[160:163]\n"
    "ds_read_b128 v[108:111], v1 offset:43008\n"
    "v_cvt_pk_bf16_f32 v138, v172, v173\n"
    "v_mfma_f32_16x16x32_bf16 v[152:155], v[112:115], v[56:59], v[152:155]\n"
    "v_cvt_pk_bf16_f32 v139, v174, v175\n"
    "v_mfma_f32_16x16x32_bf16 v[156:159], v[112:115], v[60:63], v[156:159]\n"
    "v_mfma_f32_16x16x32_bf16 v[160:163], v[112:115], v[64:67], v[160:163]\n"
    "ds_read_b128 v[112:115], v0 offset:43136\n"
    "v_mfma_f32_16x16x32_bf16 v[152:155], v[116:119], v[68:71], v[152:155]\n"
    "v_mfma_f32_16x16x32_bf16 v[156:159], v[116:119], v[72:75], v[156:159]\n"
    "v_mfma_f32_16x16x32_bf16 v[160:163], v[116:119], v[76:79], v[160:163]\n"
    "ds_read_b128 v[116:119], v1 offset:43136\n"
    "s_waitcnt lgkmcnt(2)\n"
    "v_mfma_f32_16x16x32_bf16 v[152:155], v[120:123], v[80:83], v[152:155]\n"
    "v_mfma_f32_16x16x32_bf16 v[156:159], v[120:123], v[84:87], v[156:159]\n"
    "v_mfma_f32_16x16x32_bf16 v[160:163], v[120:123], v[88:91], v[160:163]\n"
    "v_mfma_f32_16x16x32_bf16 v[152:155], v[124:127], v[92:95], v[152:155]\n"
    "v_mfma_f32_16x16x32_bf16 v[156:159], v[124:127], v[96:99], v[156:159]\n"
    "v_mfma_f32_16x16x32_bf16 v[160:163], v[124:127], v[100:103], v[160:163]\n"
    "s_nop 5\n"
    "v_cvt_pk_bf16_f32 v140, v152, v153\n"
    "v_cvt_pk_bf16_f32 v141, v154, v155\n"
    "v_cvt_pk_bf16_f32 v144, v156, v157\n"
    "v_cvt_pk_bf16_f32 v145, v158, v159\n"
    "v_cvt_pk_bf16_f32 v148, v160, v161\n"
    "v_cvt_pk_bf16_f32 v149, v162, v163\n"
    "s_waitcnt lgkmcnt(0)\n"
    "s_barrier\n"
    "ds_read_b128 v[176:179], v4 offset:8192\n"
    "ds_read_b128 v[180:183], v4 offset:9216\n"
    "v_mfma_f32_16x16x32_bf16 v[152:155], v[104:107], v[32:35], 0\n"
    "ds_read_b128 v[184:187], v4 offset:10240\n"
    "ds_read_b128 v[188:191], v4 offset:11264\n"
    "v_mfma_f32_16x16x32_bf16 v[156:159], v[104:107], v[36:39], 0\n"
    "ds_read_b128 v[192:195], v4 offset:12288\n"
    "ds_read_b128 v[196:199], v4 offset:13312\n"
    "v_mfma_f32_16x16x32_bf16 v[160:163], v[104:107], v[40:43], 0\n"
    "ds_read_b128 v[200:203], v4 offset:14336\n"
    "ds_read_b128 v[204:207], v4 offset:15360\n"
    "v_mfma_f32_16x16x32_bf16 v[152:155], v[108:111], v[44:47], v[152:155]\n"
    "ds_read_b128 v[244:247], v3 offset:0\n"
    "ds_read_b128 v[248:251], v3 offset:4096\n"
    "v_mfma_f32_16x16x32_bf16 v[156:159], v[108:111], v[48:51], v[156:159]\n"
    "ds_read_b128 v[252:255], v3 offset:8192\n"
    "ds_read_b128 v[164:167], v3 offset:12288\n"
    "v_mfma_f32_16x16x32_bf16 v[160:163], v[108:111], v[52:55], v[160:163]\n"
    "ds_read_b128 v[168:171], v3 offset:16384\n"
    "v_mfma_f32_16x16x32_bf16 v[152:155], v[112:115], v[56:59], v[152:155]\n"
    "v_mfma_f32_16x16x32_bf16 v[156:159], v[112:115], v[60:63], v[156:159]\n"
    "v_mfma_f32_16x16x32_bf16 v[160:163], v[112:115], v[64:67], v[160:163]\n"
    "s_waitcnt lgkmcnt(5)\n"
    "v_mfma_f32_16x16x32_bf16 v[208:211], v[128:131], v[176:179], 0\n"
    "s_setprio 2\n"
    "v_mfma_f32_16x16x32_bf16 v[212:215], v[132:135], v[176:179], 0\n"
    "v_mfma_f32_16x16x32_bf16 v[216:219], v[136:139], v[176:179], 0\n"
    "v_mfma_f32_16x16x32_bf16 v[208:211], v[140:143], v[180:183], v[208:211]\n"
    "v_mfma_f32_16x16x32_bf16 v[212:215], v[144:147], v[180:183], v[212:215]\n"
    "s_waitcnt lgkmcnt(0)\n"
    "global_store_dwordx4 v5, v[244:247], s[50:51] offset:0 sc1\n"
    "v_mfma_f32_16x16x32_bf16 v[216:219], v[148:151], v[180:183], v[216:219]\n"
    "v_mfma_f32_16x16x32_bf16 v[220:223], v[128:131], v[184:187], 0\n"
    "global_store_dwordx4 v6, v[248:251], s[50:51] offset:0 sc1\n"
    "v_mfma_f32_16x16x32_bf16 v[224:227], v[132:135], v[184:187], 0\n"
    "v_mfma_f32_16x16x32_bf16 v[228:231], v[136:139], v[184:187], 0\n"
    "global_store_dwordx4 v7, v[252:255], s[50:51] offset:0 sc1\n"
    "v_mfma_f32_16x16x32_bf16 v[220:223], v[140:143], v[188:191], v[220:223]\n"
    "v_cvt_pk_bf16_f32 v208, v208, v209\n"
    "v_cvt_pk_bf16_f32 v209, v210, v211\n"
    "v_mfma_f32_16x16x32_bf16 v[224:227], v[144:147], v[188:191], v[224:227]\n"
    "global_store_dwordx4 v8, v[164:167], s[50:51] offset:0 sc1\n"
    "v_mfma_f32_16x16x32_bf16 v[228:231], v[148:151], v[188:191], v[228:231]\n"
    "v_cvt_pk_bf16_f32 v210, v212, v213\n"
    "v_cvt_pk_bf16_f32 v211, v214, v215\n"
    "v_mfma_f32_16x16x32_bf16 v[232:235], v[128:131], v[192:195], 0\n"
    "s_mov_b64 exec, s[58:59]\n"
    "global_store_dwordx4 v9, v[168:171], s[50:51] offset:0 sc1\n"
    "s_mov_b64 exec, -1\n"
    "v_mfma_f32_16x16x32_bf16 v[236:239], v[132:135], v[192:195], 0\n"
    "ds_read_b128 v[120:123], v0 offset:43264\n"
    "v_cvt_pk_bf16_f32 v216, v216, v217\n"
    "v_mfma_f32_16x16x32_bf16 v[240:243], v[136:139], v[192:195], 0\n"
    "v_cvt_pk_bf16_f32 v217, v218, v219\n"
    "v_mfma_f32_16x16x32_bf16 v[232:235], v[140:143], v[196:199], v[232:235]\n"
    "s_mov_b32 exec_lo, 0xfffefffe\n"
    "s_mov_b32 exec_hi, 0xfffefffe\n"
    "ds_write_b128 v31, v[208:211] offset:0\n"
    "ds_write_b64 v16, v[216:217] offset:0\n"
    "s_mov_b64 exec, -1\n"
    "v_mfma_f32_16x16x32_bf16 v[236:239], v[144:147], v[196:199], v[236:239]\n"
    "ds_read_b128 v[124:127], v1 offset:43264\n"
    "v_cvt_pk_bf16_f32 v220, v220, v221\n"
    "v_mfma_f32_16x16x32_bf16 v[240:243], v[148:151], v[196:199], v[240:243]\n"
    "v_cvt_pk_bf16_f32 v221, v222, v223\n"
    "v_cvt_pk_bf16_f32 v222, v224, v225\n"
    "v_mfma_f32_16x16x32_bf16 v[208:211], v[128:131], v[200:203], 0\n"
    "v_cvt_pk_bf16_f32 v223, v226, v227\n"
    "v_mfma_f32_16x16x32_bf16 v[212:215], v[132:135], v[200:203], 0\n"
    "ds_read_b128 v[104:107], v0 offset:49152\n"
    "v_mfma_f32_16x16x32_bf16 v[216:219], v[136:139], v[200:203], 0\n"
    "ds_write_b128 v31, v[220:223] offset:6144\n"
    "v_mfma_f32_16x16x32_bf16 v[208:211], v[140:143], v[204:207], v[208:211]\n"
    "v_cvt_pk_bf16_f32 v228, v228, v229\n"
    "v_cvt_pk_bf16_f32 v229, v230, v231\n"
    "v_mfma_f32_16x16x32_bf16 v[212:215], v[144:147], v[204:207], v[212:215]\n"
    "ds_read_b128 v[108:111], v1 offset:49152\n"
    "v_mfma_f32_16x16x32_bf16 v[216:219], v[148:151], v[204:207], v[216:219]\n"
    "ds_write_b64 v16, v[228:229] offset:6144\n"
    "v_mfma_f32_16x16x32_bf16 v[152:155], v[116:119], v[68:71], v[152:155]\n"
    "v_cvt_pk_bf16_f32 v232, v232, v233\n"
    "v_cvt_pk_bf16_f32 v233, v234, v235\n"
    "v_mfma_f32_16x16x32_bf16 v[156:159], v[116:119], v[72:75], v[156:159]\n"
    "ds_read_b128 v[112:115], v0 offset:49280\n"
    "v_cvt_pk_bf16_f32 v234, v236, v237\n"
    "v_mfma_f32_16x16x32_bf16 v[160:163], v[116:119], v[76:79], v[160:163]\n"
    "v_cvt_pk_bf16_f32 v235, v238, v239\n"
    "s_waitcnt lgkmcnt(3)\n"
    "v_mfma_f32_16x16x32_bf16 v[152:155], v[120:123], v[80:83], v[152:155]\n"
    "ds_write_b128 v31, v[232:235] offset:12288\n"
    "v_mfma_f32_16x16x32_bf16 v[156:159], v[120:123], v[84:87], v[156:159]\n"
    "ds_read_b128 v[116:119], v1 offset:49280\n"
    "v_cvt_pk_bf16_f32 v240, v240, v241\n"
    "v_mfma_f32_16x16x32_bf16 v[160:163], v[120:123], v[88:91], v[160:163]\n"
    "v_cvt_pk_bf16_f32 v241, v242, v243\n"
    "v_mfma_f32_16x16x32_bf16 v[152:155], v[124:127], v[92:95], v[152:155]\n"
    "ds_write_b64 v16, v[240:241] offset:12288\n"
    "v_mfma_f32_16x16x32_bf16 v[156:159], v[124:127], v[96:99], v[156:159]\n"
    "ds_read_b128 v[120:123], v0 offset:49408\n"
    "v_cvt_pk_bf16_f32 v208, v208, v209\n"
    "v_mfma_f32_16x16x32_bf16 v[160:163], v[124:127], v[100:103], v[160:163]\n"
    "v_cvt_pk_bf16_f32 v209, v210, v211\n"
    "v_cvt_pk_bf16_f32 v210, v212, v213\n"
    "v_mfma_f32_16x16x32_bf16 v[164:167], v[104:107], v[32:35], 0\n"
    "v_cvt_pk_bf16_f32 v211, v214, v215\n"
    "v_cvt_pk_bf16_f32 v216, v216, v217\n"
    "v_mfma_f32_16x16x32_bf16 v[168:171], v[104:107], v[36:39], 0\n"
    "ds_read_b128 v[124:127], v1 offset:49408\n"
    "v_cvt_pk_bf16_f32 v217, v218, v219\n"
    "v_mfma_f32_16x16x32_bf16 v[172:175], v[104:107], v[40:43], 0\n"
    "s_mov_b32 exec_lo, 0x30003\n"
    "s_mov_b32 exec_hi, 0x30003\n"
    "ds_write_b128 v31, v[208:211] offset:18432\n"
    "ds_write_b64 v16, v[216:217] offset:18432\n"
    "s_mov_b64 exec, -1\n"
    "s_waitcnt lgkmcnt(5)\n"
    "v_mfma_f32_16x16x32_bf16 v[164:167], v[108:111], v[44:47], v[164:167]\n"
    "s_setprio 0\n"
    "v_lshlrev_b32 v30, 8, v2\n"
    "s_mov_b64 exec, s[70:71]\n"
    "global_atomic_add v30, v4, s[72:73] sc1\n"
    "s_mov_b64 exec, -1\n"
    "v_mfma_f32_16x16x32_bf16 v[168:171], v[108:111], v[48:51], v[168:171]\n"
    "ds_read_b128 v[104:107], v0 offset:55296\n"
    "v_cvt_pk_bf16_f32 v142, v152, v153\n"
    "v_mfma_f32_16x16x32_bf16 v[172:175], v[108:111], v[52:55], v[172:175]\n"
    "v_cvt_pk_bf16_f32 v143, v154, v155\n"
    "v_cvt_pk_bf16_f32 v146, v156, v157\n"
    "v_mfma_f32_16x16x32_bf16 v[164:167], v[112:115], v[56:59], v[164:167]\n"
    "v_cvt_pk_bf16_f32 v147, v158, v159\n"
    "v_cvt_pk_bf16_f32 v150, v160, v161\n"
    "v_mfma_f32_16x16x32_bf16 v[168:171], v[112:115], v[60:63], v[168:171]\n"
    "ds_read_b128 v[108:111], v1 offset:55296\n"
    "v_cvt_pk_bf16_f32 v151, v162, v163\n"
    "v_mfma_f32_16x16x32_bf16 v[172:175], v[112:115], v[64:67], v[172:175]\n"
    "v_add_u32 v30, 96, v11\n"
    "v_add_u32 v10, 18432, v10\n"
    "v_mfma_f32_16x16x32_bf16 v[164:167], v[116:119], v[68:71], v[164:167]\n"
    "v_and_b32 v30, 0x70, v30\n"
    "v_xad_u32 v31, v30, v12, v10\n"
    "v_mfma_f32_16x16x32_bf16 v[168:171], v[116:119], v[72:75], v[168:171]\n"
    "ds_read_b128 v[112:115], v0 offset:55424\n"
    "v_xad_u32 v16, v30, v13, v10\n"
    "v_mfma_f32_16x16x32_bf16 v[172:175], v[116:119], v[76:79], v[172:175]\n"
    "s_waitcnt lgkmcnt(2)\n"
    "v_mfma_f32_16x16x32_bf16 v[164:167], v[120:123], v[80:83], v[164:167]\n"
    "v_mfma_f32_16x16x32_bf16 v[168:171], v[120:123], v[84:87], v[168:171]\n"
    "ds_read_b128 v[116:119], v1 offset:55424\n"
    "v_mfma_f32_16x16x32_bf16 v[172:175], v[120:123], v[88:91], v[172:175]\n"
    "v_mfma_f32_16x16x32_bf16 v[164:167], v[124:127], v[92:95], v[164:167]\n"
    "v_mfma_f32_16x16x32_bf16 v[168:171], v[124:127], v[96:99], v[168:171]\n"
    "ds_read_b128 v[120:123], v0 offset:55552\n"
    "v_mfma_f32_16x16x32_bf16 v[172:175], v[124:127], v[100:103], v[172:175]\n"
    "v_mfma_f32_16x16x32_bf16 v[152:155], v[104:107], v[32:35], 0\n"
    "v_mfma_f32_16x16x32_bf16 v[156:159], v[104:107], v[36:39], 0\n"
    "ds_read_b128 v[124:127], v1 offset:55552\n"
    "v_mfma_f32_16x16x32_bf16 v[160:163], v[104:107], v[40:43], 0\n"
    "ds_read_b128 v[104:107], v14 offset:0\n"
    "s_waitcnt lgkmcnt(3)\n"
    "v_mfma_f32_16x16x32_bf16 v[152:155], v[108:111], v[44:47], v[152:155]\n"
    "v_cvt_pk_bf16_f32 v128, v164, v165\n"
    "v_cvt_pk_bf16_f32 v129, v166, v167\n"
    "v_mfma_f32_16x16x32_bf16 v[156:159], v[108:111], v[48:51], v[156:159]\n"
    "v_cvt_pk_bf16_f32 v132, v168, v169\n"
    "v_cvt_pk_bf16_f32 v133, v170, v171\n"
    "v_mfma_f32_16x16x32_bf16 v[160:163], v[108:111], v[52:55], v[160:163]\n"
    "ds_read_b128 v[108:111], v15 offset:0\n"
    "v_cvt_pk_bf16_f32 v136, v172, v173\n"
    "v_mfma_f32_16x16x32_bf16 v[152:155], v[112:115], v[56:59], v[152:155]\n"
    "v_cvt_pk_bf16_f32 v137, v174, v175\n"
    "v_mfma_f32_16x16x32_bf16 v[156:159], v[112:115], v[60:63], v[156:159]\n"
    "v_mfma_f32_16x16x32_bf16 v[160:163], v[112:115], v[64:67], v[160:163]\n"
    "ds_read_b128 v[112:115], v14 offset:128\n"
    "v_mfma_f32_16x16x32_bf16 v[152:155], v[116:119], v[68:71], v[152:155]\n"
    "v_mfma_f32_16x16x32_bf16 v[156:159], v[116:119], v[72:75], v[156:159]\n"
    "v_mfma_f32_16x16x32_bf16 v[160:163], v[116:119], v[76:79], v[160:163]\n"
    "ds_read_b128 v[116:119], v15 offset:128\n"
    "s_waitcnt lgkmcnt(2)\n"
    "v_mfma_f32_16x16x32_bf16 v[152:155], v[120:123], v[80:83], v[152:155]\n"
    "v_mfma_f32_16x16x32_bf16 v[156:159], v[120:123], v[84:87], v[156:159]\n"
    "v_mfma_f32_16x16x32_bf16 v[160:163], v[120:123], v[88:91], v[160:163]\n"
    "v_mfma_f32_16x16x32_bf16 v[152:155], v[124:127], v[92:95], v[152:155]\n"
    "v_mfma_f32_16x16x32_bf16 v[156:159], v[124:127], v[96:99], v[156:159]\n"
    "v_mfma_f32_16x16x32_bf16 v[160:163], v[124:127], v[100:103], v[160:163]\n"
    "s_nop 5\n"
    "v_cvt_pk_bf16_f32 v130, v152, v153\n"
    "v_cvt_pk_bf16_f32 v131, v154, v155\n"
    "v_cvt_pk_bf16_f32 v134, v156, v157\n"
    "v_cvt_pk_bf16_f32 v135, v158, v159\n"
    "v_cvt_pk_bf16_f32 v138, v160, v161\n"
    "v_cvt_pk_bf16_f32 v139, v162, v163\n"
    "s_waitcnt lgkmcnt(0)\n"
    "s_barrier\n"
    "ds_read_b128 v[176:179], v4 offset:0\n"
    "ds_read_b128 v[180:183], v4 offset:1024\n"
    "v_mfma_f32_16x16x32_bf16 v[152:155], v[104:107], v[32:35], 0\n"
    "ds_read_b128 v[184:187], v4 offset:2048\n"
    "ds_read_b128 v[188:191], v4 offset:3072\n"
    "v_mfma_f32_16x16x32_bf16 v[156:159], v[104:107], v[36:39], 0\n"
    "ds_read_b128 v[192:195], v4 offset:4096\n"
    "ds_read_b128 v[196:199], v4 offset:5120\n"
    "v_mfma_f32_16x16x32_bf16 v[160:163], v[104:107], v[40:43], 0\n"
    "ds_read_b128 v[200:203], v4 offset:6144\n"
    "ds_read_b128 v[204:207], v4 offset:7168\n"
    "v_mfma_f32_16x16x32_bf16 v[152:155], v[108:111], v[44:47], v[152:155]\n"
    "ds_read_b128 v[244:247], v3 offset:18816\n"
    "ds_read_b128 v[248:251], v3 offset:22912\n"
    "v_mfma_f32_16x16x32_bf16 v[156:159], v[108:111], v[48:51], v[156:159]\n"
    "ds_read_b128 v[252:255], v3 offset:27008\n"
    "ds_read_b128 v[164:167], v3 offset:31104\n"
    "v_mfma_f32_16x16x32_bf16 v[160:163], v[108:111], v[52:55], v[160:163]\n"
    "ds_read_b128 v[168:171], v3 offset:35200\n"
    "v_mfma_f32_16x16x32_bf16 v[152:155], v[112:115], v[56:59], v[152:155]\n"
    "v_mfma_f32_16x16x32_bf16 v[156:159], v[112:115], v[60:63], v[156:159]\n"
    "v_mfma_f32_16x16x32_bf16 v[160:163], v[112:115], v[64:67], v[160:163]\n"
    "s_waitcnt lgkmcnt(5)\n"
    "v_mfma_f32_16x16x32_bf16 v[208:211], v[128:131], v[176:179], 0\n"
    "s_setprio 2\n"
    "v_mfma_f32_16x16x32_bf16 v[212:215], v[132:135], v[176:179], 0\n"
    "v_mfma_f32_16x16x32_bf16 v[216:219], v[136:139], v[176:179], 0\n"
    "v_mfma_f32_16x16x32_bf16 v[208:211], v[140:143], v[180:183], v[208:211]\n"
    "v_mfma_f32_16x16x32_bf16 v[212:215], v[144:147], v[180:183], v[212:215]\n"
    "s_waitcnt lgkmcnt(0)\n"
    "global_store_dwordx4 v5, v[244:247], s[50:51] offset:2688 sc1\n"
    "v_mfma_f32_16x16x32_bf16 v[216:219], v[148:151], v[180:183], v[216:219]\n"
    "v_mfma_f32_16x16x32_bf16 v[220:223], v[128:131], v[184:187], 0\n"
    "global_store_dwordx4 v6, v[248:251], s[50:51] offset:2688 sc1\n"
    "v_mfma_f32_16x16x32_bf16 v[224:227], v[132:135], v[184:187], 0\n"
    "v_mfma_f32_16x16x32_bf16 v[228:231], v[136:139], v[184:187], 0\n"
    "global_store_dwordx4 v7, v[252:255], s[50:51] offset:2688 sc1\n"
    "v_mfma_f32_16x16x32_bf16 v[220:223], v[140:143], v[188:191], v[220:223]\n"
    "v_cvt_pk_bf16_f32 v208, v208, v209\n"
    "v_cvt_pk_bf16_f32 v209, v210, v211\n"
    "v_mfma_f32_16x16x32_bf16 v[224:227], v[144:147], v[188:191], v[224:227]\n"
    "global_store_dwordx4 v8, v[164:167], s[50:51] offset:2688 sc1\n"
    "v_mfma_f32_16x16x32_bf16 v[228:231], v[148:151], v[188:191], v[228:231]\n"
    "v_cvt_pk_bf16_f32 v210, v212, v213\n"
    "v_cvt_pk_bf16_f32 v211, v214, v215\n"
    "v_mfma_f32_16x16x32_bf16 v[232:235], v[128:131], v[192:195], 0\n"
    "s_mov_b64 exec, s[58:59]\n"
    "global_store_dwordx4 v9, v[168:171], s[50:51] offset:2688 sc1\n"
    "s_mov_b64 exec, -1\n"
    "v_mfma_f32_16x16x32_bf16 v[236:239], v[132:135], v[192:195], 0\n"
    "ds_read_b128 v[120:123], v14 offset:256\n"
    "v_cvt_pk_bf16_f32 v216, v216, v217\n"
    "v_mfma_f32_16x16x32_bf16 v[240:243], v[136:139], v[192:195], 0\n"
    "v_cvt_pk_bf16_f32 v217, v218, v219\n"
    "v_mfma_f32_16x16x32_bf16 v[232:235], v[140:143], v[196:199], v[232:235]\n"
    "s_mov_b32 exec_lo, 0xfffcfffc\n"
    "s_mov_b32 exec_hi, 0xfffcfffc\n"
    "ds_write_b128 v31, v[208:211] offset:0\n"
    "ds_write_b64 v16, v[216:217] offset:0\n"
    "s_mov_b64 exec, -1\n"
    "v_mfma_f32_16x16x32_bf16 v[236:239], v[144:147], v[196:199], v[236:239]\n"
    "ds_read_b128 v[124:127], v15 offset:256\n"
    "v_cvt_pk_bf16_f32 v220, v220, v221\n"
    "v_mfma_f32_16x16x32_bf16 v[240:243], v[148:151], v[196:199], v[240:243]\n"
    "v_cvt_pk_bf16_f32 v221, v222, v223\n"
    "v_cvt_pk_bf16_f32 v222, v224, v225\n"
    "v_mfma_f32_16x16x32_bf16 v[208:211], v[128:131], v[200:203], 0\n"
    "v_cvt_pk_bf16_f32 v223, v226, v227\n"
    "v_mfma_f32_16x16x32_bf16 v[212:215], v[132:135], v[200:203], 0\n"
    "ds_read_b128 v[104:107], v14 offset:6144\n"
    "v_mfma_f32_16x16x32_bf16 v[216:219], v[136:139], v[200:203], 0\n"
    "ds_write_b128 v31, v[220:223] offset:6144\n"
    "v_mfma_f32_16x16x32_bf16 v[208:211], v[140:143], v[204:207], v[208:211]\n"
    "v_cvt_pk_bf16_f32 v228, v228, v229\n"
    "v_cvt_pk_bf16_f32 v229, v230, v231\n"
    "v_mfma_f32_16x16x32_bf16 v[212:215], v[144:147], v[204:207], v[212:215]\n"
    "ds_read_b128 v[108:111], v15 offset:6144\n"
    "v_mfma_f32_16x16x32_bf16 v[216:219], v[148:151], v[204:207], v[216:219]\n"
    "ds_write_b64 v16, v[228:229] offset:6144\n"
    "v_mfma_f32_16x16x32_bf16 v[152:155], v[116:119], v[68:71], v[152:155]\n"
    "v_cvt_pk_bf16_f32 v232, v232, v233\n"
    "v_cvt_pk_bf16_f32 v233, v234, v235\n"
    "v_mfma_f32_16x16x32_bf16 v[156:159], v[116:119], v[72:75], v[156:159]\n"
    "ds_read_b128 v[112:115], v14 offset:6272\n"
    "v_cvt_pk_bf16_f32 v234, v236, v237\n"
    "v_mfma_f32_16x16x32_bf16 v[160:163], v[116:119], v[76:79], v[160:163]\n"
    "v_cvt_pk_bf16_f32 v235, v238, v239\n"
    "s_waitcnt lgkmcnt(3)\n"
    "v_mfma_f32_16x16x32_bf16 v[152:155], v[120:123], v[80:83], v[152:155]\n"
    "ds_write_b128 v31, v[232:235] offset:12288\n"
    "v_mfma_f32_16x16x32_bf16 v[156:159], v[120:123], v[84:87], v[156:159]\n"
    "ds_read_b128 v[116:119], v15 offset:6272\n"
    "v_cvt_pk_bf16_f32 v240, v240, v241\n"
    "v_mfma_f32_16x16x32_bf16 v[160:163], v[120:123], v[88:91], v[160:163]\n"
    "v_cvt_pk_bf16_f32 v241, v242, v243\n"
    "v_mfma_f32_16x16x32_bf16 v[152:155], v[124:127], v[92:95], v[152:155]\n"
    "ds_write_b64 v16, v[240:241] offset:12288\n"
    "v_mfma_f32_16x16x32_bf16 v[156:159], v[124:127], v[96:99], v[156:159]\n"
    "ds_read_b128 v[120:123], v14 offset:6400\n"
    "v_cvt_pk_bf16_f32 v208, v208, v209\n"
    "v_mfma_f32_16x16x32_bf16 v[160:163], v[124:127], v[100:103], v[160:163]\n"
    "v_cvt_pk_bf16_f32 v209, v210, v211\n"
    "v_cvt_pk_bf16_f32 v210, v212, v213\n"
    "v_mfma_f32_16x16x32_bf16 v[164:167], v[104:107], v[32:35], 0\n"
    "v_cvt_pk_bf16_f32 v211, v214, v215\n"
    "v_cvt_pk_bf16_f32 v216, v216, v217\n"
    "v_mfma_f32_16x16x32_bf16 v[168:171], v[104:107], v[36:39], 0\n"
    "ds_read_b128 v[124:127], v15 offset:6400\n"
    "v_cvt_pk_bf16_f32 v217, v218, v219\n"
    "v_mfma_f32_16x16x32_bf16 v[172:175], v[104:107], v[40:43], 0\n"
    "s_mov_b32 exec_lo, 0x70007\n"
    "s_mov_b32 exec_hi, 0x70007\n"
    "ds_write_b128 v31, v[208:211] offset:18432\n"
    "ds_write_b64 v16, v[216:217] offset:18432\n"
    "s_mov_b64 exec, -1\n"
    "s_waitcnt lgkmcnt(5)\n"
    "v_mfma_f32_16x16x32_bf16 v[164:167], v[108:111], v[44:47], v[164:167]\n"
    "s_setprio 0\n"
    "v_lshlrev_b32 v30, 8, v2\n"
    "s_mov_b64 exec, s[70:71]\n"
    "global_atomic_add v30, v4, s[72:73] sc1\n"
    "s_mov_b64 exec, -1\n"
    "v_mfma_f32_16x16x32_bf16 v[168:171], v[108:111], v[48:51], v[168:171]\n"
    "ds_read_b128 v[104:107], v14 offset:12288\n"
    "v_cvt_pk_bf16_f32 v140, v152, v153\n"
    "v_mfma_f32_16x16x32_bf16 v[172:175], v[108:111], v[52:55], v[172:175]\n"
    "v_cvt_pk_bf16_f32 v141, v154, v155\n"
    "v_cvt_pk_bf16_f32 v144, v156, v157\n"
    "v_mfma_f32_16x16x32_bf16 v[164:167], v[112:115], v[56:59], v[164:167]\n"
    "v_cvt_pk_bf16_f32 v145, v158, v159\n"
    "v_cvt_pk_bf16_f32 v148, v160, v161\n"
    "v_mfma_f32_16x16x32_bf16 v[168:171], v[112:115], v[60:63], v[168:171]\n"
    "ds_read_b128 v[108:111], v15 offset:12288\n"
    "v_cvt_pk_bf16_f32 v149, v162, v163\n"
    "v_mfma_f32_16x16x32_bf16 v[172:175], v[112:115], v[64:67], v[172:175]\n"
    "ds_read_b128 v[112:115], v14 offset:12416\n"
    "v_add_u32 v30, 80, v11\n"
    "v_mfma_f32_16x16x32_bf16 v[164:167], v[116:119], v[68:71], v[164:167]\n"
    "v_add_u32 v10, 18432, v10\n"
    "v_and_b32 v30, 0x70, v30\n"
    "v_mfma_f32_16x16x32_bf16 v[168:171], v[116:119], v[72:75], v[168:171]\n"
    "v_xad_u32 v31, v30, v12, v10\n"
    "v_xad_u32 v16, v30, v13, v10\n"
    "v_mfma_f32_16x16x32_bf16 v[172:175], v[116:119], v[76:79], v[172:175]\n"
    "ds_read_b128 v[116:119], v15 offset:12416\n"
    "s_waitcnt lgkmcnt(3)\n"
    "v_mfma_f32_16x16x32_bf16 v[164:167], v[120:123], v[80:83], v[164:167]\n"
    "v_mfma_f32_16x16x32_bf16 v[168:171], v[120:123], v[84:87], v[168:171]\n"
    "v_mfma_f32_16x16x32_bf16 v[172:175], v[120:123], v[88:91], v[172:175]\n"
    "ds_read_b128 v[120:123], v14 offset:12544\n"
    "v_mfma_f32_16x16x32_bf16 v[164:167], v[124:127], v[92:95], v[164:167]\n"
    "v_mfma_f32_16x16x32_bf16 v[168:171], v[124:127], v[96:99], v[168:171]\n"
    "v_mfma_f32_16x16x32_bf16 v[172:175], v[124:127], v[100:103], v[172:175]\n"
    "ds_read_b128 v[124:127], v15 offset:12544\n"
    "v_mfma_f32_16x16x32_bf16 v[152:155], v[104:107], v[32:35], 0\n"
    "v_mfma_f32_16x16x32_bf16 v[156:159], v[104:107], v[36:39], 0\n"
    "v_mfma_f32_16x16x32_bf16 v[160:163], v[104:107], v[40:43], 0\n"
    "s_waitcnt lgkmcnt(1)\n"
    "v_mfma_f32_16x16x32_bf16 v[152:155], v[108:111], v[44:47], v[152:155]\n"
    "s_waitcnt lgkmcnt(0)\n"
    "s_barrier\n"
    "v_cvt_pk_bf16_f32 v142, v164, v165\n"
    "v_cvt_pk_bf16_f32 v143, v166, v167\n"
    "v_mfma_f32_16x16x32_bf16 v[156:159], v[108:111], v[48:51], v[156:159]\n"
    "ds_read_b128 v[180:183], v4 offset:9216\n"
    "v_cvt_pk_bf16_f32 v146, v168, v169\n"
    "v_mfma_f32_16x16x32_bf16 v[160:163], v[108:111], v[52:55], v[160:163]\n"
    "ds_read_b128 v[176:179], v4 offset:8192\n"
    "v_cvt_pk_bf16_f32 v147, v170, v171\n"
    "v_mfma_f32_16x16x32_bf16 v[152:155], v[112:115], v[56:59], v[152:155]\n"
    "ds_read_b128 v[188:191], v4 offset:11264\n"
    "v_cvt_pk_bf16_f32 v150, v172, v173\n"
    "v_mfma_f32_16x16x32_bf16 v[156:159], v[112:115], v[60:63], v[156:159]\n"
    "ds_read_b128 v[184:187], v4 offset:10240\n"
    "v_cvt_pk_bf16_f32 v151, v174, v175\n"
    "v_mfma_f32_16x16x32_bf16 v[160:163], v[112:115], v[64:67], v[160:163]\n"
    "ds_read_b128 v[196:199], v4 offset:13312\n"
    "v_mfma_f32_16x16x32_bf16 v[152:155], v[116:119], v[68:71], v[152:155]\n"
    "ds_read_b128 v[192:195], v4 offset:12288\n"
    "v_mfma_f32_16x16x32_bf16 v[156:159], v[116:119], v[72:75], v[156:159]\n"
    "ds_read_b128 v[204:207], v4 offset:15360\n"
    "v_mfma_f32_16x16x32_bf16 v[160:163], v[116:119], v[76:79], v[160:163]\n"
    "ds_read_b128 v[200:203], v4 offset:14336\n"
    "v_mfma_f32_16x16x32_bf16 v[152:155], v[120:123], v[80:83], v[152:155]\n"
    "v_mfma_f32_16x16x32_bf16 v[156:159], v[120:123], v[84:87], v[156:159]\n"
    "v_mfma_f32_16x16x32_bf16 v[160:163], v[120:123], v[88:91], v[160:163]\n"
    "v_mfma_f32_16x16x32_bf16 v[152:155], v[124:127], v[92:95], v[152:155]\n"
    "v_mfma_f32_16x16x32_bf16 v[156:159], v[124:127], v[96:99], v[156:159]\n"
    "v_mfma_f32_16x16x32_bf16 v[160:163], v[124:127], v[100:103], v[160:163]\n"
    "s_waitcnt lgkmcnt(0)\n"
    "v_mfma_f32_16x16x32_bf16 v[208:211], v[140:143], v[180:183], 0\n"
    "v_mfma_f32_16x16x32_bf16 v[212:215], v[144:147], v[180:183], 0\n"
    "v_mfma_f32_16x16x32_bf16 v[216:219], v[148:151], v[180:183], 0\n"
    "s_nop 1\n"
    "v_cvt_pk_bf16_f32 v128, v152, v153\n"
    "v_cvt_pk_bf16_f32 v129, v154, v155\n"
    "v_cvt_pk_bf16_f32 v132, v156, v157\n"
    "v_mfma_f32_16x16x32_bf16 v[220:223], v[140:143], v[188:191], 0\n"
    "v_cvt_pk_bf16_f32 v133, v158, v159\n"
    "v_cvt_pk_bf16_f32 v136, v160, v161\n"
    "v_cvt_pk_bf16_f32 v137, v162, v163\n"
    "v_mfma_f32_16x16x32_bf16 v[224:227], v[144:147], v[188:191], 0\n"
    "v_mfma_f32_16x16x32_bf16 v[228:231], v[148:151], v[188:191], 0\n"
    "v_mfma_f32_16x16x32_bf16 v[232:235], v[140:143], v[196:199], 0\n"
    "v_mfma_f32_16x16x32_bf16 v[236:239], v[144:147], v[196:199], 0\n"
    "v_mfma_f32_16x16x32_bf16 v[240:243], v[148:151], v[196:199], 0\n"
    "v_mfma_f32_16x16x32_bf16 v[208:211], v[128:131], v[176:179], v[208:211]\n"
    "v_mfma_f32_16x16x32_bf16 v[212:215], v[132:135], v[176:179], v[212:215]\n"
    "v_mfma_f32_16x16x32_bf16 v[216:219], v[136:139], v[176:179], v[216:219]\n"
    "v_mfma_f32_16x16x32_bf16 v[220:223], v[128:131], v[184:187], v[220:223]\n"
    "v_mfma_f32_16x16x32_bf16 v[224:227], v[132:135], v[184:187], v[224:227]\n"
    "v_mfma_f32_16x16x32_bf16 v[228:231], v[136:139], v[184:187], v[228:231]\n"
    "s_nop 2\n"
    "v_cvt_pk_bf16_f32 v208, v208, v209\n"
    "v_cvt_pk_bf16_f32 v209, v210, v211\n"
    "v_cvt_pk_bf16_f32 v210, v212, v213\n"
    "v_cvt_pk_bf16_f32 v211, v214, v215\n"
    "v_mfma_f32_16x16x32_bf16 v[232:235], v[128:131], v[192:195], v[232:235]\n"
    "v_cvt_pk_bf16_f32 v216, v216, v217\n"
    "v_cvt_pk_bf16_f32 v217, v218, v219\n"
    "s_mov_b32 exec_lo, 0xfff8fff8\n"
    "s_mov_b32 exec_hi, 0xfff8fff8\n"
    "ds_write_b128 v31, v[208:211] offset:0\n"
    "ds_write_b64 v16, v[216:217] offset:0\n"
    "s_mov_b64 exec, -1\n"
    "v_mfma_f32_16x16x32_bf16 v[236:239], v[132:135], v[192:195], v[236:239]\n"
    "v_mfma_f32_16x16x32_bf16 v[240:243], v[136:139], v[192:195], v[240:243]\n"
    "v_cvt_pk_bf16_f32 v220, v220, v221\n"
    "v_cvt_pk_bf16_f32 v221, v222, v223\n"
    "v_cvt_pk_bf16_f32 v222, v224, v225\n"
    "v_cvt_pk_bf16_f32 v223, v226, v227\n"
    "v_mfma_f32_16x16x32_bf16 v[208:211], v[140:143], v[204:207], 0\n"
    "ds_write_b128 v31, v[220:223] offset:6144\n"
    "v_cvt_pk_bf16_f32 v228, v228, v229\n"
    "v_cvt_pk_bf16_f32 v229, v230, v231\n"
    "v_mfma_f32_16x16x32_bf16 v[212:215], v[144:147], v[204:207], 0\n"
    "ds_write_b64 v16, v[228:229] offset:6144\n"
    "v_mfma_f32_16x16x32_bf16 v[216:219], v[148:151], v[204:207], 0\n"
    "v_cvt_pk_bf16_f32 v232, v232, v233\n"
    "v_cvt_pk_bf16_f32 v233, v234, v235\n"
    "v_cvt_pk_bf16_f32 v234, v236, v237\n"
    "v_cvt_pk_bf16_f32 v235, v238, v239\n"
    "v_mfma_f32_16x16x32_bf16 v[208:211], v[128:131], v[200:203], v[208:211]\n"
    "ds_write_b128 v31, v[232:235] offset:12288\n"
    "v_cvt_pk_bf16_f32 v240, v240, v241\n"
    "v_cvt_pk_bf16_f32 v241, v242, v243\n"
    "v_mfma_f32_16x16x32_bf16 v[212:215], v[132:135], v[200:203], v[212:215]\n"
    "ds_write_b64 v16, v[240:241] offset:12288\n"
    "v_mfma_f32_16x16x32_bf16 v[216:219], v[136:139], v[200:203], v[216:219]\n"
    "s_nop 1\n"
    "v_cvt_pk_bf16_f32 v208, v208, v209\n"
    "v_cvt_pk_bf16_f32 v209, v210, v211\n"
    "s_nop 1\n"
    "v_cvt_pk_bf16_f32 v210, v212, v213\n"
    "v_cvt_pk_bf16_f32 v211, v214, v215\n"
    "v_cvt_pk_bf16_f32 v216, v216, v217\n"
    "v_cvt_pk_bf16_f32 v217, v218, v219\n"
    "s_mov_b32 exec_lo, 0xf000f\n"
    "s_mov_b32 exec_hi, 0xf000f\n"
    "ds_write_b128 v31, v[208:211] offset:18432\n"
    "ds_write_b64 v16, v[216:217] offset:18432\n"
    "s_mov_b64 exec, -1\n"
    "s_add_u32 s66, s50, 8064\n"
    "s_addc_u32 s67, s51, 0\n"
    "s_waitcnt lgkmcnt(0)\n"
    "s_barrier\n"
    "ds_read_b128 v[244:247], v3 offset:56448\n"
    "ds_read_b128 v[248:251], v3 offset:60544\n"
    "s_waitcnt lgkmcnt(1)\n"
    "global_store_dwordx4 v5, v[244:247], s[66:67] offset:0 sc1\n"
    "s_waitcnt lgkmcnt(0)\n"
    "global_store_dwordx4 v6, v[248:251], s[66:67] offset:0 sc1\n"
    "s_endpgm\n"
    "Lanp_%=:\n"
    "s_waitcnt lgkmcnt(0)\n"
    "s_barrier\n"
    "s_cmp_eq_u32 s3, 3\n"
    "s_cbranch_scc0 Lasetup_%=\n"
    "v_lshlrev_b32 v33, 4, v32\n"
    "v_add_u32 v33, 76800, v33\n"
    "v_mov_b32 v36, 0\n"
    "v_mov_b32 v37, 0\n"
    "v_mov_b32 v38, 0\n"
    "v_mov_b32 v39, 0\n"
    "ds_write_b128 v33, v[36:39] offset:0\n"
    "ds_write_b128 v33, v[36:39] offset:1024\n"
    "ds_write_b128 v33, v[36:39] offset:2048\n"
    "Lasetup_%=:\n"
    "s_and_b32 s49, s3, 3\n"
    "s_and_b32 s23, s2, 7\n"
    "s_lshl_b32 s22, s23, 2\n"
    "s_lshr_b32 s24, s2, 6\n"
    "s_add_u32 s22, s22, s24\n"
    "s_mul_i32 s22, s22, 0x93000\n"
    "s_and_b32 s24, s19, 7\n"
    "s_mul_i32 s24, s24, 0x2a00\n"
    "s_add_u32 s22, s22, s24\n"
    "s_lshl_b32 s48, s49, 10\n"
    "v_and_b32 v33, 15, v32\n"
    "v_lshrrev_b32 v34, 4, v32\n"
    "v_lshlrev_b32 v35, 4, v32\n"
    "v_lshrrev_b32 v36, 1, v33\n"
    "v_mul_u32_u24 v37, 0x180, v33\n"
    "v_xor_b32 v0, v34, v36\n"
    "v_xor_b32 v1, 4, v0\n"
    "v_lshl_add_u32 v0, v0, 4, v37\n"
    "v_lshl_add_u32 v1, v1, 4, v37\n"
    "s_add_u32 s50, s12, s22\n"
    "s_addc_u32 s51, s13, 0\n"
    "s_mul_i32 s24, s3, 0x4800\n"
    "s_add_u32 s24, s24, 0x1000\n"
    "s_add_u32 s40, s10, s24\n"
    "s_addc_u32 s41, s11, 0\n"
    "s_add_u32 s42, s40, 0x2000\n"
    "s_addc_u32 s43, s41, 0\n"
    "s_add_u32 s44, s40, 0x4000\n"
    "s_addc_u32 s45, s41, 0\n"
    "s_cmp_eq_u32 s49, 2\n"
    "s_cselect_b32 s24, 0xffffff, 0\n"
    "s_cmp_lt_u32 s49, 2\n"
    "s_cselect_b32 s24, -1, s24\n"
    "s_cselect_b32 s25, -1, 0\n"
    "s_mov_b64 s[56:57], s[24:25]\n"
    "s_add_u32 s50, s50, 5376\n"
    "s_addc_u32 s51, s51, 0\n"
    "v_add_u32 v5, 79872, v35\n"
    "v_add_u32 v2, s48, v5\n"
    "s_lshr_b32 s24, s49, 1\n"
    "s_lshl_b32 s24, s24, 10\n"
    "s_and_b32 s25, s49, 1\n"
    "s_lshl_b32 s25, s25, 3\n"
    "s_add_u32 s24, s24, s25\n"
    "s_add_u32 s24, s24, 0x1000\n"
    "v_add_u32 v3, s24, v5\n"
    "s_lshl_b32 s24, s49, 11\n"
    "s_add_u32 s24, s24, 129024\n"
    "v_add_u32 v4, s24, v35\n"
    "v_mov_b32 v6, v35\n"
    "v_cmp_eq_u32 vcc, 0, v34\n"
    "v_mov_b32 v36, 0xff800000\n"
    "v_mov_b32 v37, 0\n"
    "s_nop 0\n"
    "v_cndmask_b32 v7, v37, v36, vcc\n"
    "v_cndmask_b32 v8, v36, v37, vcc\n"
    "s_mul_i32 s24, s49, 0x1800\n"
    "v_add_u32 v11, s24, v5\n"
    "v_add_u32 v12, 0xf000, v0\n"
    "v_add_u32 v13, 0xf000, v1\n"
    "s_lshl_b32 s24, s49, 11\n"
    "s_add_u32 s24, s24, 145408\n"
    "v_lshlrev_b32 v14, 5, v32\n"
    "v_add_u32 v14, s24, v14\n"
    "s_setprio 0\n"
    "s_waitcnt lgkmcnt(0)\n"
    "s_barrier\n"
    "global_load_dwordx4 v[16:19], v6, s[40:41] offset:-4096 sc1\n"
    "global_load_dwordx4 v[20:23], v6, s[40:41] offset:-3072 sc1\n"
    "global_load_dwordx4 v[28:31], v6, s[40:41] offset:-1024 sc1\n"
    "global_load_dwordx4 v[32:35], v6, s[40:41] offset:0 sc1\n"
    "global_load_dwordx4 v[40:43], v6, s[40:41] offset:2048 sc1\n"
    "global_load_dwordx4 v[44:47], v6, s[40:41] offset:3072 sc1\n"
    "global_load_dwordx4 v[52:55], v6, s[42:43] offset:-3072 sc1\n"
    "global_load_dwordx4 v[56:59], v6, s[42:43] offset:-2048 sc1\n"
    "global_load_dwordx4 v[64:67], v6, s[42:43] offset:0 sc1\n"
    "global_load_dwordx4 v[68:71], v6, s[42:43] offset:1024 sc1\n"
    "global_load_dwordx4 v[76:79], v6, s[42:43] offset:3072 sc1\n"
    "global_load_dwordx4 v[80:83], v6, s[44:45] offset:-4096 sc1\n"
    "ds_read_b128 v[88:91], v0 offset:0\n"
    "ds_read_b128 v[112:115], v0 offset:6144\n"
    "ds_read_b128 v[136:139], v0 offset:12288\n"
    "ds_read_b128 v[160:163], v0 offset:18432\n"
    "ds_read_b128 v[92:95], v1 offset:0\n"
    "ds_read_b128 v[116:119], v1 offset:6144\n"
    "ds_read_b128 v[140:143], v1 offset:12288\n"
    "ds_read_b128 v[164:167], v1 offset:18432\n"
    "ds_read_b128 v[96:99], v0 offset:128\n"
    "ds_read_b128 v[120:123], v0 offset:6272\n"
    "ds_read_b128 v[144:147], v0 offset:12416\n"
    "ds_read_b128 v[168:171], v0 offset:18560\n"
    "ds_read_b128 v[100:103], v1 offset:128\n"
    "ds_read_b128 v[124:127], v1 offset:6272\n"
    "ds_read_b128 v[148:151], v1 offset:12416\n"
    "ds_read_b128 v[172:175], v1 offset:18560\n"
    "ds_read_b128 v[104:107], v0 offset:256\n"
    "ds_read_b128 v[128:131], v0 offset:6400\n"
    "ds_read_b128 v[152:155], v0 offset:12544\n"
    "ds_read_b128 v[176:179], v0 offset:18688\n"
    "ds_read_b128 v[108:111], v1 offset:256\n"
    "ds_read_b128 v[132:135], v1 offset:6400\n"
    "ds_read_b128 v[156:159], v1 offset:12544\n"
    "ds_read_b128 v[180:183], v1 offset:18688\n"
    "v_mov_b32 v236, 0x3f803f80\n"
    "v_mov_b32 v240, 0\n"
    "v_mov_b32 v244, v8\n"
    "v_mov_b32 v237, 0x3f803f80\n"
    "v_mov_b32 v241, 0\n"
    "v_mov_b32 v245, v8\n"
    "v_mov_b32 v238, 0x3f803f80\n"
    "v_mov_b32 v242, 0\n"
    "v_mov_b32 v246, v8\n"
    "v_mov_b32 v239, 0x3f803f80\n"
    "v_mov_b32 v243, 0\n"
    "v_mov_b32 v247, v8\n"
    "v_mov_b32 v9, 0x28000\n"
    "s_cmp_eq_u32 s49, 0\n"
    "s_cselect_b32 s74, 0xffffffff, -1\n"
    "s_cmp_eq_u32 s49, 3\n"
    "s_cselect_b32 s74, 0x10001, s74\n"
    "s_mov_b32 s75, s74\n"
    "v_cndmask_b32 v11, v9, v11, s[74:75]\n"
    "s_waitcnt vmcnt(10)\n"
    "s_waitcnt lgkmcnt(15)\n"
    "v_mfma_f32_16x16x32_bf16 v[184:187], v[16:19], v[88:91], 0\n"
    "v_mfma_f32_16x16x32_bf16 v[188:191], v[20:23], v[88:91], 0\n"
    "global_load_dwordx4 v[24:27], v6, s[40:41] offset:-2048 sc1\n"
    "v_mfma_f32_16x16x32_bf16 v[196:199], v[16:19], v[112:115], 0\n"
    "v_mfma_f32_16x16x32_bf16 v[200:203], v[20:23], v[112:115], 0\n"
    "v_mfma_f32_16x16x32_bf16 v[208:211], v[16:19], v[136:139], 0\n"
    "v_mfma_f32_16x16x32_bf16 v[212:215], v[20:23], v[136:139], 0\n"
    "global_load_dwordx4 v[36:39], v6, s[40:41] offset:1024 sc1\n"
    "v_mfma_f32_16x16x32_bf16 v[220:223], v[16:19], v[160:163], 0\n"
    "v_mfma_f32_16x16x32_bf16 v[224:227], v[20:23], v[160:163], 0\n"
    "s_waitcnt vmcnt(10)\n"
    "v_mfma_f32_16x16x32_bf16 v[184:187], v[28:31], v[92:95], v[184:187]\n"
    "v_mfma_f32_16x16x32_bf16 v[188:191], v[32:35], v[92:95], v[188:191]\n"
    "global_load_dwordx4 v[48:51], v6, s[42:43] offset:-4096 sc1\n"
    "v_mfma_f32_16x16x32_bf16 v[196:199], v[28:31], v[116:119], v[196:199]\n"
    "v_mfma_f32_16x16x32_bf16 v[200:203], v[32:35], v[116:119], v[200:203]\n"
    "v_mfma_f32_16x16x32_bf16 v[208:211], v[28:31], v[140:143], v[208:211]\n"
    "v_mfma_f32_16x16x32_bf16 v[212:215], v[32:35], v[140:143], v[212:215]\n"
    "global_load_dwordx4 v[60:63], v6, s[42:43] offset:-1024 sc1\n"
    "v_mfma_f32_16x16x32_bf16 v[220:223], v[28:31], v[164:167], v[220:223]\n"
    "v_mfma_f32_16x16x32_bf16 v[224:227], v[32:35], v[164:167], v[224:227]\n"
    "s_waitcnt vmcnt(10)\n"
    "v_mfma_f32_16x16x32_bf16 v[184:187], v[40:43], v[96:99], v[184:187]\n"
    "v_mfma_f32_16x16x32_bf16 v[188:191], v[44:47], v[96:99], v[188:191]\n"
    "global_load_dwordx4 v[72:75], v6, s[42:43] offset:2048 sc1\n"
    "s_waitcnt lgkmcnt(0)\n"
    "v_mfma_f32_16x16x32_bf16 v[196:199], v[40:43], v[120:123], v[196:199]\n"
    "v_mfma_f32_16x16x32_bf16 v[200:203], v[44:47], v[120:123], v[200:203]\n"
    "v_mfma_f32_16x16x32_bf16 v[208:211], v[40:43], v[144:147], v[208:211]\n"
    "v_mfma_f32_16x16x32_bf16 v[212:215], v[44:47], v[144:147], v[212:215]\n"
    "global_load_dwordx4 v[84:87], v6, s[44:45] offset:-3072 sc1\n"
    "s_barrier\n"
    "v_mfma_f32_16x16x32_bf16 v[220:223], v[40:43], v[168:171], v[220:223]\n"
    "v_mfma_f32_16x16x32_bf16 v[224:227], v[44:47], v[168:171], v[224:227]\n"
    "s_waitcnt vmcnt(10)\n"
    "v_mfma_f32_16x16x32_bf16 v[184:187], v[52:55], v[100:103], v[184:187]\n"
    "v_mfma_f32_16x16x32_bf16 v[188:191], v[56:59], v[100:103], v[188:191]\n"
    "v_mfma_f32_16x16x32_bf16 v[196:199], v[52:55], v[124:127], v[196:199]\n"
    "v_mfma_f32_16x16x32_bf16 v[200:203], v[56:59], v[124:127], v[200:203]\n"
    "v_mfma_f32_16x16x32_bf16 v[208:211], v[52:55], v[148:151], v[208:211]\n"
    "v_mfma_f32_16x16x32_bf16 v[212:215], v[56:59], v[148:151], v[212:215]\n"
    "v_mfma_f32_16x16x32_bf16 v[220:223], v[52:55], v[172:175], v[220:223]\n"
    "v_mfma_f32_16x16x32_bf16 v[224:227], v[56:59], v[172:175], v[224:227]\n"
    "s_waitcnt vmcnt(8)\n"
    "v_mfma_f32_16x16x32_bf16 v[184:187], v[64:67], v[104:107], v[184:187]\n"
    "v_mfma_f32_16x16x32_bf16 v[188:191], v[68:71], v[104:107], v[188:191]\n"
    "v_mfma_f32_16x16x32_bf16 v[196:199], v[64:67], v[128:131], v[196:199]\n"
    "v_mfma_f32_16x16x32_bf16 v[200:203], v[68:71], v[128:131], v[200:203]\n"
    "v_mfma_f32_16x16x32_bf16 v[208:211], v[64:67], v[152:155], v[208:211]\n"
    "v_mfma_f32_16x16x32_bf16 v[212:215], v[68:71], v[152:155], v[212:215]\n"
    "v_mfma_f32_16x16x32_bf16 v[220:223], v[64:67], v[176:179], v[220:223]\n"
    "v_mfma_f32_16x16x32_bf16 v[224:227], v[68:71], v[176:179], v[224:227]\n"
    "s_waitcnt vmcnt(6)\n"
    "v_mfma_f32_16x16x32_bf16 v[184:187], v[76:79], v[108:111], v[184:187]\n"
    "v_mfma_f32_16x16x32_bf16 v[188:191], v[80:83], v[108:111], v[188:191]\n"
    "v_mfma_f32_16x16x32_bf16 v[196:199], v[76:79], v[132:135], v[196:199]\n"
    "v_mfma_f32_16x16x32_bf16 v[200:203], v[80:83], v[132:135], v[200:203]\n"
    "v_mfma_f32_16x16x32_bf16 v[208:211], v[76:79], v[156:159], v[208:211]\n"
    "v_mfma_f32_16x16x32_bf16 v[212:215], v[80:83], v[156:159], v[212:215]\n"
    "v_mfma_f32_16x16x32_bf16 v[220:223], v[76:79], v[180:183], v[220:223]\n"
    "v_mfma_f32_16x16x32_bf16 v[224:227], v[80:83], v[180:183], v[224:227]\n"
    "s_waitcnt vmcnt(5)\n"
    "v_mfma_f32_16x16x32_bf16 v[192:195], v[24:27], v[88:91], 0\n"
    "v_mfma_f32_16x16x32_bf16 v[204:207], v[24:27], v[112:115], 0\n"
    "v_cvt_pk_bf16_f32 v184, v184, v185\n"
    "v_cvt_pk_bf16_f32 v185, v186, v187\n"
    "v_mfma_f32_16x16x32_bf16 v[216:219], v[24:27], v[136:139], 0\n"
    "v_cvt_pk_bf16_f32 v186, v188, v189\n"
    "v_cvt_pk_bf16_f32 v187, v190, v191\n"
    "v_mfma_f32_16x16x32_bf16 v[228:231], v[24:27], v[160:163], 0\n"
    "ds_write_b128 v2, v[184:187] offset:0\n"
    "s_waitcnt vmcnt(4)\n"
    "v_mfma_f32_16x16x32_bf16 v[192:195], v[36:39], v[92:95], v[192:195]\n"
    "v_cvt_pk_bf16_f32 v196, v196, v197\n"
    "v_cvt_pk_bf16_f32 v197, v198, v199\n"
    "v_mfma_f32_16x16x32_bf16 v[204:207], v[36:39], v[116:119], v[204:207]\n"
    "v_cvt_pk_bf16_f32 v198, v200, v201\n"
    "v_cvt_pk_bf16_f32 v199, v202, v203\n"
    "v_mfma_f32_16x16x32_bf16 v[216:219], v[36:39], v[140:143], v[216:219]\n"
    "ds_write_b128 v2, v[196:199] offset:6144\n"
    "v_mfma_f32_16x16x32_bf16 v[228:231], v[36:39], v[164:167], v[228:231]\n"
    "v_cvt_pk_bf16_f32 v208, v208, v209\n"
    "v_cvt_pk_bf16_f32 v209, v210, v211\n"
    "s_waitcnt vmcnt(3)\n"
    "v_mfma_f32_16x16x32_bf16 v[192:195], v[48:51], v[96:99], v[192:195]\n"
    "v_cvt_pk_bf16_f32 v210, v212, v213\n"
    "v_cvt_pk_bf16_f32 v211, v214, v215\n"
    "v_mfma_f32_16x16x32_bf16 v[204:207], v[48:51], v[120:123], v[204:207]\n"
    "ds_write_b128 v2, v[208:211] offset:12288\n"
    "v_mfma_f32_16x16x32_bf16 v[216:219], v[48:51], v[144:147], v[216:219]\n"
    "v_cvt_pk_bf16_f32 v220, v220, v221\n"
    "v_cvt_pk_bf16_f32 v221, v222, v223\n"
    "v_mfma_f32_16x16x32_bf16 v[228:231], v[48:51], v[168:171], v[228:231]\n"
    "v_cvt_pk_bf16_f32 v222, v224, v225\n"
    "v_cvt_pk_bf16_f32 v223, v226, v227\n"
    "s_waitcnt vmcnt(2)\n"
    "v_mfma_f32_16x16x32_bf16 v[192:195], v[60:63], v[100:103], v[192:195]\n"
    "ds_write_b128 v2, v[220:223] offset:18432\n"
    "v_mfma_f32_16x16x32_bf16 v[204:207], v[60:63], v[124:127], v[204:207]\n"
    "v_mfma_f32_16x16x32_bf16 v[216:219], v[60:63], v[148:151], v[216:219]\n"
    "v_mfma_f32_16x16x32_bf16 v[228:231], v[60:63], v[172:175], v[228:231]\n"
    "s_waitcnt vmcnt(1)\n"
    "v_mfma_f32_16x16x32_bf16 v[192:195], v[72:75], v[104:107], v[192:195]\n"
    "v_mfma_f32_16x16x32_bf16 v[204:207], v[72:75], v[128:131], v[204:207]\n"
    "v_mfma_f32_16x16x32_bf16 v[216:219], v[72:75], v[152:155], v[216:219]\n"
    "v_mfma_f32_16x16x32_bf16 v[228:231], v[72:75], v[176:179], v[228:231]\n"
    "s_waitcnt vmcnt(0)\n"
    "v_mfma_f32_16x16x32_bf16 v[192:195], v[84:87], v[108:111], v[192:195]\n"
    "v_mfma_f32_16x16x32_bf16 v[204:207], v[84:87], v[132:135], v[204:207]\n"
    "v_mfma_f32_16x16x32_bf16 v[216:219], v[84:87], v[156:159], v[216:219]\n"
    "v_mfma_f32_16x16x32_bf16 v[228:231], v[84:87], v[180:183], v[228:231]\n"
    "s_nop 4\n"
    "v_cvt_pk_bf16_f32 v192, v192, v193\n"
    "v_cvt_pk_bf16_f32 v193, v194, v195\n"
    "ds_write_b64 v3, v[192:193] offset:0\n"
    "v_cvt_pk_bf16_f32 v204, v204, v205\n"
    "v_cvt_pk_bf16_f32 v205, v206, v207\n"
    "ds_write_b64 v3, v[204:205] offset:6144\n"
    "v_cvt_pk_bf16_f32 v216, v216, v217\n"
    "v_cvt_pk_bf16_f32 v217, v218, v219\n"
    "ds_write_b64 v3, v[216:217] offset:12288\n"
    "v_cvt_pk_bf16_f32 v228, v228, v229\n"
    "v_cvt_pk_bf16_f32 v229, v230, v231\n"
    "ds_write_b64 v3, v[228:229] offset:18432\n"
    "s_waitcnt lgkmcnt(0)\n"
    "s_barrier\n"
    "ds_read_b128 v[184:187], v11 offset:0\n"
    "ds_read_b128 v[188:191], v11 offset:1024\n"
    "ds_read_b128 v[192:195], v11 offset:2048\n"
    "ds_read_b128 v[196:199], v11 offset:3072\n"
    "ds_read_b128 v[200:203], v11 offset:4096\n"
    "ds_read_b128 v[204:207], v11 offset:5120\n"
    "s_waitcnt lgkmcnt(5)\n"
    "v_mfma_f32_16x16x32_bf16 v[208:211], v[88:91], v[184:187], 0\n"
    "v_mfma_f32_16x16x32_bf16 v[212:215], v[112:115], v[184:187], 0\n"
    "v_mfma_f32_16x16x32_bf16 v[216:219], v[136:139], v[184:187], 0\n"
    "v_mfma_f32_16x16x32_bf16 v[220:223], v[160:163], v[184:187], v[244:247]\n"
    "s_waitcnt lgkmcnt(4)\n"
    "v_mfma_f32_16x16x32_bf16 v[208:211], v[92:95], v[188:191], v[208:211]\n"
    "v_mfma_f32_16x16x32_bf16 v[212:215], v[116:119], v[188:191], v[212:215]\n"
    "v_mfma_f32_16x16x32_bf16 v[216:219], v[140:143], v[188:191], v[216:219]\n"
    "v_mfma_f32_16x16x32_bf16 v[220:223], v[164:167], v[188:191], v[220:223]\n"
    "s_waitcnt lgkmcnt(0)\n"
    "v_mfma_f32_16x16x32_bf16 v[208:211], v[96:99], v[192:195], v[208:211]\n"
    "v_mfma_f32_16x16x32_bf16 v[212:215], v[120:123], v[192:195], v[212:215]\n"
    "v_mfma_f32_16x16x32_bf16 v[216:219], v[144:147], v[192:195], v[216:219]\n"
    "v_mfma_f32_16x16x32_bf16 v[220:223], v[168:171], v[192:195], v[220:223]\n"
    "v_mfma_f32_16x16x32_bf16 v[208:211], v[100:103], v[196:199], v[208:211]\n"
    "v_mfma_f32_16x16x32_bf16 v[212:215], v[124:127], v[196:199], v[212:215]\n"
    "v_mfma_f32_16x16x32_bf16 v[216:219], v[148:151], v[196:199], v[216:219]\n"
    "v_mfma_f32_16x16x32_bf16 v[220:223], v[172:175], v[196:199], v[220:223]\n"
    "v_mfma_f32_16x16x32_bf16 v[208:211], v[104:107], v[200:203], v[208:211]\n"
    "v_mfma_f32_16x16x32_bf16 v[212:215], v[128:131], v[200:203], v[212:215]\n"
    "v_mfma_f32_16x16x32_bf16 v[216:219], v[152:155], v[200:203], v[216:219]\n"
    "v_mfma_f32_16x16x32_bf16 v[220:223], v[176:179], v[200:203], v[220:223]\n"
    "v_mfma_f32_16x16x32_bf16 v[208:211], v[108:111], v[204:207], v[208:211]\n"
    "v_mfma_f32_16x16x32_bf16 v[212:215], v[132:135], v[204:207], v[212:215]\n"
    "v_mfma_f32_16x16x32_bf16 v[216:219], v[156:159], v[204:207], v[216:219]\n"
    "v_mfma_f32_16x16x32_bf16 v[220:223], v[180:183], v[204:207], v[220:223]\n"
    "s_nop 4\n"
    "v_max3_f32 v224, v208, v209, v210\n"
    "v_max3_f32 v225, v211, v212, v213\n"
    "v_max3_f32 v226, v214, v215, v216\n"
    "v_max3_f32 v227, v217, v218, v219\n"
    "v_max3_f32 v228, v224, v225, v226\n"
    "v_max3_f32 v229, v228, v227, v220\n"
    "v_mov_b32 v249, v229\n"
    "s_nop 1\n"
    "v_permlane16_swap_b32 v229, v249\n"
    "v_max_f32 v248, v229, v249\n"
    "v_mov_b32 v249, v248\n"
    "s_nop 1\n"
    "v_permlane32_swap_b32 v248, v249\n"
    "v_max_f32 v248, v248, v249\n"
    "v_sub_f32 v208, v208, v248\n"
    "v_sub_f32 v209, v209, v248\n"
    "v_sub_f32 v210, v210, v248\n"
    "v_sub_f32 v211, v211, v248\n"
    "v_sub_f32 v212, v212, v248\n"
    "v_sub_f32 v213, v213, v248\n"
    "v_sub_f32 v214, v214, v248\n"
    "v_sub_f32 v215, v215, v248\n"
    "v_sub_f32 v216, v216, v248\n"
    "v_sub_f32 v217, v217, v248\n"
    "v_sub_f32 v218, v218, v248\n"
    "v_sub_f32 v219, v219, v248\n"
    "v_sub_f32 v220, v220, v248\n"
    "v_exp_f32 v208, v208\n"
    "v_exp_f32 v209, v209\n"
    "v_exp_f32 v210, v210\n"
    "v_exp_f32 v211, v211\n"
    "v_exp_f32 v212, v212\n"
    "v_exp_f32 v213, v213\n"
    "v_exp_f32 v214, v214\n"
    "v_exp_f32 v215, v215\n"
    "v_exp_f32 v216, v216\n"
    "v_exp_f32 v217, v217\n"
    "v_exp_f32 v218, v218\n"
    "v_exp_f32 v219, v219\n"
    "v_exp_f32 v220, v220\n"
    "s_barrier\n"
    "ds_read_b128 v[88:91], v0 offset:24576\n"
    "ds_read_b128 v[92:95], v1 offset:24576\n"
    "ds_read_b128 v[96:99], v0 offset:24704\n"
    "ds_read_b128 v[100:103], v1 offset:24704\n"
    "ds_read_b128 v[104:107], v0 offset:24832\n"
    "ds_read_b128 v[108:111], v1 offset:24832\n"
    "v_cvt_pk_bf16_f32 v224, v208, v209\n"
    "v_cvt_pk_bf16_f32 v225, v210, v211\n"
    "v_cvt_pk_bf16_f32 v226, v212, v213\n"
    "v_cvt_pk_bf16_f32 v227, v214, v215\n"
    "v_cvt_pk_bf16_f32 v228, v216, v217\n"
    "v_cvt_pk_bf16_f32 v229, v218, v219\n"
    "v_cvt_pk_bf16_f32 v230, v220, 0\n"
    "v_mov_b32 v231, 0\n"
    "s_waitcnt lgkmcnt(5)\n"
    "v_mfma_f32_16x16x32_bf16 v[184:187], v[16:19], v[88:91], 0\n"
    "v_mfma_f32_16x16x32_bf16 v[188:191], v[20:23], v[88:91], 0\n"
    "ds_read_b128 v[112:115], v0 offset:30720\n"
    "v_mfma_f32_16x16x32_bf16 v[232:235], v[236:239], v[224:227], 0\n"
    "v_mfma_f32_16x16x32_bf16 v[232:235], v[236:239], v[228:231], v[232:235]\n"
    "v_mfma_f32_16x16x32_bf16 v[192:195], v[24:27], v[88:91], 0\n"
    "s_waitcnt lgkmcnt(5)\n"
    "v_mfma_f32_16x16x32_bf16 v[184:187], v[28:31], v[92:95], v[184:187]\n"
    "ds_read_b128 v[116:119], v1 offset:30720\n"
    "v_mfma_f32_16x16x32_bf16 v[188:191], v[32:35], v[92:95], v[188:191]\n"
    "v_mfma_f32_16x16x32_bf16 v[192:195], v[36:39], v[92:95], v[192:195]\n"
    "ds_read_b128 v[120:123], v0 offset:30848\n"
    "s_nop 0\n"
    "v_rcp_f32 v250, v232\n"
    "s_waitcnt lgkmcnt(2)\n"
    "v_mfma_f32_16x16x32_bf16 v[184:187], v[40:43], v[96:99], v[184:187]\n"
    "v_cndmask_b32 v250, 0, v250, s[74:75]\n"
    "v_mul_f32 v208, v208, v250\n"
    "v_mul_f32 v209, v209, v250\n"
    "v_mfma_f32_16x16x32_bf16 v[188:191], v[44:47], v[96:99], v[188:191]\n"
    "ds_read_b128 v[124:127], v1 offset:30848\n"
    "v_mul_f32 v210, v210, v250\n"
    "v_mul_f32 v211, v211, v250\n"
    "v_mul_f32 v212, v212, v250\n"
    "v_mfma_f32_16x16x32_bf16 v[192:195], v[48:51], v[96:99], v[192:195]\n"
    "v_mul_f32 v213, v213, v250\n"
    "v_mul_f32 v214, v214, v250\n"
    "v_mul_f32 v215, v215, v250\n"
    "v_mfma_f32_16x16x32_bf16 v[184:187], v[52:55], v[100:103], v[184:187]\n"
    "ds_read_b128 v[128:131], v0 offset:30976\n"
    "v_mul_f32 v216, v216, v250\n"
    "v_mul_f32 v217, v217, v250\n"
    "v_mul_f32 v218, v218, v250\n"
    "v_mfma_f32_16x16x32_bf16 v[188:191], v[56:59], v[100:103], v[188:191]\n"
    "v_mul_f32 v219, v219, v250\n"
    "v_mul_f32 v220, v220, v250\n"
    "v_cvt_pk_bf16_f32 v224, v208, v209\n"
    "v_mfma_f32_16x16x32_bf16 v[192:195], v[60:63], v[100:103], v[192:195]\n"
    "ds_read_b128 v[132:135], v1 offset:30976\n"
    "v_cvt_pk_bf16_f32 v225, v210, v211\n"
    "v_cvt_pk_bf16_f32 v226, v212, v213\n"
    "v_cvt_pk_bf16_f32 v227, v214, v215\n"
    "v_mfma_f32_16x16x32_bf16 v[184:187], v[64:67], v[104:107], v[184:187]\n"
    "ds_write_b128 v4, v[224:227] offset:0\n"
    "v_cvt_pk_bf16_f32 v228, v216, v217\n"
    "v_mfma_f32_16x16x32_bf16 v[188:191], v[68:71], v[104:107], v[188:191]\n"
    "v_cvt_pk_bf16_f32 v229, v218, v219\n"
    "v_cvt_pk_bf16_f32 v230, v220, 0\n"
    "v_mfma_f32_16x16x32_bf16 v[192:195], v[72:75], v[104:107], v[192:195]\n"
    "ds_read_b128 v[136:139], v0 offset:36864\n"
    "ds_write_b128 v4, v[228:231] offset:1024\n"
    "v_mfma_f32_16x16x32_bf16 v[184:187], v[76:79], v[108:111], v[184:187]\n"
    "v_mfma_f32_16x16x32_bf16 v[188:191], v[80:83], v[108:111], v[188:191]\n"
    "v_mfma_f32_16x16x32_bf16 v[192:195], v[84:87], v[108:111], v[192:195]\n"
    "ds_read_b128 v[140:143], v1 offset:36864\n"
    "v_mfma_f32_16x16x32_bf16 v[196:199], v[16:19], v[112:115], 0\n"
    "v_mfma_f32_16x16x32_bf16 v[200:203], v[20:23], v[112:115], 0\n"
    "v_mfma_f32_16x16x32_bf16 v[204:207], v[24:27], v[112:115], 0\n"
    "ds_read_b128 v[144:147], v0 offset:36992\n"
    "s_waitcnt lgkmcnt(2)\n"
    "v_mfma_f32_16x16x32_bf16 v[196:199], v[28:31], v[116:119], v[196:199]\n"
    "v_cvt_pk_bf16_f32 v184, v184, v185\n"
    "v_cvt_pk_bf16_f32 v185, v186, v187\n"
    "v_cvt_pk_bf16_f32 v186, v188, v189\n"
    "v_mfma_f32_16x16x32_bf16 v[200:203], v[32:35], v[116:119], v[200:203]\n"
    "v_cvt_pk_bf16_f32 v187, v190, v191\n"
    "ds_write_b128 v2, v[184:187] offset:24576\n"
    "v_mfma_f32_16x16x32_bf16 v[204:207], v[36:39], v[116:119], v[204:207]\n"
    "ds_read_b128 v[148:151], v1 offset:36992\n"
    "v_cvt_pk_bf16_f32 v192, v192, v193\n"
    "v_cvt_pk_bf16_f32 v193, v194, v195\n"
    "v_mfma_f32_16x16x32_bf16 v[196:199], v[40:43], v[120:123], v[196:199]\n"
    "ds_write_b64 v3, v[192:193] offset:24576\n"
    "v_mfma_f32_16x16x32_bf16 v[200:203], v[44:47], v[120:123], v[200:203]\n"
    "v_mfma_f32_16x16x32_bf16 v[204:207], v[48:51], v[120:123], v[204:207]\n"
    "ds_read_b128 v[152:155], v0 offset:37120\n"
    "v_mfma_f32_16x16x32_bf16 v[196:199], v[52:55], v[124:127], v[196:199]\n"
    "v_mfma_f32_16x16x32_bf16 v[200:203], v[56:59], v[124:127], v[200:203]\n"
    "v_mfma_f32_16x16x32_bf16 v[204:207], v[60:63], v[124:127], v[204:207]\n"
    "ds_read_b128 v[156:159], v1 offset:37120\n"
    "v_mfma_f32_16x16x32_bf16 v[196:199], v[64:67], v[128:131], v[196:199]\n"
    "v_mfma_f32_16x16x32_bf16 v[200:203], v[68:71], v[128:131], v[200:203]\n"
    "v_mfma_f32_16x16x32_bf16 v[204:207], v[72:75], v[128:131], v[204:207]\n"
    "v_mfma_f32_16x16x32_bf16 v[196:199], v[76:79], v[132:135], v[196:199]\n"
    "v_mfma_f32_16x16x32_bf16 v[200:203], v[80:83], v[132:135], v[200:203]\n"
    "v_mfma_f32_16x16x32_bf16 v[204:207], v[84:87], v[132:135], v[204:207]\n"
    "v_mfma_f32_16x16x32_bf16 v[184:187], v[16:19], v[136:139], 0\n"
    "v_mfma_f32_16x16x32_bf16 v[188:191], v[20:23], v[136:139], 0\n"
    "s_waitcnt lgkmcnt(0)\n"
    "v_mfma_f32_16x16x32_bf16 v[184:187], v[28:31], v[140:143], v[184:187]\n"
    "v_mfma_f32_16x16x32_bf16 v[188:191], v[32:35], v[140:143], v[188:191]\n"
    "s_nop 0\n"
    "v_cvt_pk_bf16_f32 v196, v196, v197\n"
    "v_cvt_pk_bf16_f32 v197, v198, v199\n"
    "v_cvt_pk_bf16_f32 v198, v200, v201\n"
    "v_mfma_f32_16x16x32_bf16 v[184:187], v[40:43], v[144:147], v[184:187]\n"
    "v_cvt_pk_bf16_f32 v199, v202, v203\n"
    "ds_write_b128 v2, v[196:199] offset:30720\n"
    "v_mfma_f32_16x16x32_bf16 v[188:191], v[44:47], v[144:147], v[188:191]\n"
    "v_cvt_pk_bf16_f32 v204, v204, v205\n"
    "v_cvt_pk_bf16_f32 v205, v206, v207\n"
    "v_mfma_f32_16x16x32_bf16 v[184:187], v[52:55], v[148:151], v[184:187]\n"
    "ds_write_b64 v3, v[204:205] offset:30720\n"
    "v_mfma_f32_16x16x32_bf16 v[188:191], v[56:59], v[148:151], v[188:191]\n"
    "v_mfma_f32_16x16x32_bf16 v[184:187], v[64:67], v[152:155], v[184:187]\n"
    "v_mov_b32 v240, v7\n"
    "v_mfma_f32_16x16x32_bf16 v[188:191], v[68:71], v[152:155], v[188:191]\n"
    "s_add_u32 s60, s49, 3\n"
    "s_and_b32 s60, s60, 7\n"
    "s_mul_i32 s60, s60, 0x1800\n"
    "v_add_u32 v11, s60, v5\n"
    "s_cmp_eq_u32 s49, 0\n"
    "s_cselect_b32 s76, 0xfffefffe, -1\n"
    "s_cmp_eq_u32 s49, 3\n"
    "s_cselect_b32 s76, 0x30003, s76\n"
    "s_mov_b32 s77, s76\n"
    "v_cndmask_b32 v11, v9, v11, s[76:77]\n"
    "v_mfma_f32_16x16x32_bf16 v[184:187], v[76:79], v[156:159], v[184:187]\n"
    "v_mfma_f32_16x16x32_bf16 v[188:191], v[80:83], v[156:159], v[188:191]\n"
    "v_mfma_f32_16x16x32_bf16 v[192:195], v[24:27], v[136:139], 0\n"
    "v_mfma_f32_16x16x32_bf16 v[192:195], v[36:39], v[140:143], v[192:195]\n"
    "v_mfma_f32_16x16x32_bf16 v[192:195], v[48:51], v[144:147], v[192:195]\n"
    "s_nop 3\n"
    "v_cvt_pk_bf16_f32 v184, v184, v185\n"
    "v_cvt_pk_bf16_f32 v185, v186, v187\n"
    "v_cvt_pk_bf16_f32 v186, v188, v189\n"
    "v_mfma_f32_16x16x32_bf16 v[192:195], v[60:63], v[148:151], v[192:195]\n"
    "v_cvt_pk_bf16_f32 v187, v190, v191\n"
    "ds_write_b128 v2, v[184:187] offset:36864\n"
    "v_mfma_f32_16x16x32_bf16 v[192:195], v[72:75], v[152:155], v[192:195]\n"
    "v_mfma_f32_16x16x32_bf16 v[192:195], v[84:87], v[156:159], v[192:195]\n"
    "s_nop 7\n"
    "v_cvt_pk_bf16_f32 v192, v192, v193\n"
    "v_cvt_pk_bf16_f32 v193, v194, v195\n"
    "ds_write_b64 v3, v[192:193] offset:36864\n"
    "s_waitcnt lgkmcnt(0)\n"
    "s_barrier\n"
    "ds_read_b128 v[184:187], v11 offset:0\n"
    "ds_read_b128 v[188:191], v11 offset:1024\n"
    "ds_read_b128 v[192:195], v11 offset:2048\n"
    "ds_read_b128 v[196:199], v11 offset:3072\n"
    "ds_read_b128 v[200:203], v11 offset:4096\n"
    "ds_read_b128 v[204:207], v11 offset:5120\n"
    "s_waitcnt lgkmcnt(5)\n"
    "v_mfma_f32_16x16x32_bf16 v[208:211], v[160:163], v[184:187], v[240:243]\n"
    "v_mfma_f32_16x16x32_bf16 v[212:215], v[88:91], v[184:187], 0\n"
    "v_mfma_f32_16x16x32_bf16 v[216:219], v[112:115], v[184:187], 0\n"
    "v_mfma_f32_16x16x32_bf16 v[220:223], v[136:139], v[184:187], v[244:247]\n"
    "s_waitcnt lgkmcnt(4)\n"
    "v_mfma_f32_16x16x32_bf16 v[208:211], v[164:167], v[188:191], v[208:211]\n"
    "ds_read_b128 v[160:163], v0 offset:43008\n"
    "v_mfma_f32_16x16x32_bf16 v[212:215], v[92:95], v[188:191], v[212:215]\n"
    "v_mfma_f32_16x16x32_bf16 v[216:219], v[116:119], v[188:191], v[216:219]\n"
    "ds_read_b128 v[88:91], v0 offset:49152\n"
    "v_mfma_f32_16x16x32_bf16 v[220:223], v[140:143], v[188:191], v[220:223]\n"
    "s_waitcnt lgkmcnt(2)\n"
    "v_mfma_f32_16x16x32_bf16 v[208:211], v[168:171], v[192:195], v[208:211]\n"
    "ds_read_b128 v[164:167], v1 offset:43008\n"
    "v_mfma_f32_16x16x32_bf16 v[212:215], v[96:99], v[192:195], v[212:215]\n"
    "v_mfma_f32_16x16x32_bf16 v[216:219], v[120:123], v[192:195], v[216:219]\n"
    "ds_read_b128 v[92:95], v1 offset:49152\n"
    "v_mfma_f32_16x16x32_bf16 v[220:223], v[144:147], v[192:195], v[220:223]\n"
    "v_mfma_f32_16x16x32_bf16 v[208:211], v[172:175], v[196:199], v[208:211]\n"
    "ds_read_b128 v[168:171], v0 offset:43136\n"
    "v_mfma_f32_16x16x32_bf16 v[212:215], v[100:103], v[196:199], v[212:215]\n"
    "v_mfma_f32_16x16x32_bf16 v[216:219], v[124:127], v[196:199], v[216:219]\n"
    "ds_read_b128 v[96:99], v0 offset:49280\n"
    "v_mfma_f32_16x16x32_bf16 v[220:223], v[148:151], v[196:199], v[220:223]\n"
    "v_mfma_f32_16x16x32_bf16 v[208:211], v[176:179], v[200:203], v[208:211]\n"
    "ds_read_b128 v[172:175], v1 offset:43136\n"
    "v_mfma_f32_16x16x32_bf16 v[212:215], v[104:107], v[200:203], v[212:215]\n"
    "v_mfma_f32_16x16x32_bf16 v[216:219], v[128:131], v[200:203], v[216:219]\n"
    "ds_read_b128 v[100:103], v1 offset:49280\n"
    "v_mfma_f32_16x16x32_bf16 v[220:223], v[152:155], v[200:203], v[220:223]\n"
    "v_mfma_f32_16x16x32_bf16 v[208:211], v[180:183], v[204:207], v[208:211]\n"
    "ds_read_b128 v[176:179], v0 offset:43264\n"
    "v_mfma_f32_16x16x32_bf16 v[212:215], v[108:111], v[204:207], v[212:215]\n"
    "v_mfma_f32_16x16x32_bf16 v[216:219], v[132:135], v[204:207], v[216:219]\n"
    "ds_read_b128 v[104:107], v0 offset:49408\n"
    "v_mfma_f32_16x16x32_bf16 v[220:223], v[156:159], v[204:207], v[220:223]\n"
    "s_waitcnt lgkmcnt(3)\n"
    "v_mfma_f32_16x16x32_bf16 v[184:187], v[16:19], v[160:163], 0\n"
    "ds_read_b128 v[180:183], v1 offset:43264\n"
    "v_mfma_f32_16x16x32_bf16 v[188:191], v[20:23], v[160:163], 0\n"
    "ds_read_b128 v[108:111], v1 offset:49408\n"
    "v_mfma_f32_16x16x32_bf16 v[192:195], v[24:27], v[160:163], 0\n"
    "v_max3_f32 v224, v208, v209, v210\n"
    "v_max3_f32 v225, v211, v212, v213\n"
    "v_max3_f32 v226, v214, v215, v216\n"
    "v_mfma_f32_16x16x32_bf16 v[184:187], v[28:31], v[164:167], v[184:187]\n"
    "v_max3_f32 v227, v217, v218, v219\n"
    "v_max3_f32 v228, v224, v225, v226\n"
    "v_max3_f32 v229, v227, v220, v221\n"
    "v_mfma_f32_16x16x32_bf16 v[188:191], v[32:35], v[164:167], v[188:191]\n"
    "ds_read_b128 v[112:115], v0 offset:55296\n"
    "v_max_f32 v230, v228, v229\n"
    "v_mov_b32 v249, v230\n"
    "v_mfma_f32_16x16x32_bf16 v[192:195], v[36:39], v[164:167], v[192:195]\n"
    "s_nop 0\n"
    "v_permlane16_swap_b32 v230, v249\n"
    "v_max_f32 v248, v230, v249\n"
    "v_mov_b32 v249, v248\n"
    "v_mfma_f32_16x16x32_bf16 v[184:187], v[40:43], v[168:171], v[184:187]\n"
    "v_mfma_f32_16x16x32_bf16 v[188:191], v[44:47], v[168:171], v[188:191]\n"
    "v_permlane32_swap_b32 v248, v249\n"
    "v_max_f32 v248, v248, v249\n"
    "v_sub_f32 v208, v208, v248\n"
    "v_mfma_f32_16x16x32_bf16 v[192:195], v[48:51], v[168:171], v[192:195]\n"
    "ds_read_b128 v[116:119], v1 offset:55296\n"
    "v_sub_f32 v209, v209, v248\n"
    "v_sub_f32 v210, v210, v248\n"
    "v_sub_f32 v211, v211, v248\n"
    "v_mfma_f32_16x16x32_bf16 v[184:187], v[52:55], v[172:175], v[184:187]\n"
    "v_sub_f32 v212, v212, v248\n"
    "v_sub_f32 v213, v213, v248\n"
    "v_sub_f32 v214, v214, v248\n"
    "v_mfma_f32_16x16x32_bf16 v[188:191], v[56:59], v[172:175], v[188:191]\n"
    "v_sub_f32 v215, v215, v248\n"
    "v_sub_f32 v216, v216, v248\n"
    "v_sub_f32 v217, v217, v248\n"
    "v_mfma_f32_16x16x32_bf16 v[192:195], v[60:63], v[172:175], v[192:195]\n"
    "v_sub_f32 v218, v218, v248\n"
    "v_sub_f32 v219, v219, v248\n"
    "v_sub_f32 v220, v220, v248\n"
    "s_waitcnt lgkmcnt(1)\n"
    "v_mfma_f32_16x16x32_bf16 v[184:187], v[64:67], v[176:179], v[184:187]\n"
    "ds_read_b128 v[120:123], v0 offset:55424\n"
    "v_sub_f32 v221, v221, v248\n"
    "v_exp_f32 v208, v208\n"
    "v_mfma_f32_16x16x32_bf16 v[188:191], v[68:71], v[176:179], v[188:191]\n"
    "v_exp_f32 v209, v209\n"
    "v_mfma_f32_16x16x32_bf16 v[192:195], v[72:75], v[176:179], v[192:195]\n"
    "v_exp_f32 v210, v210\n"
    "v_mfma_f32_16x16x32_bf16 v[184:187], v[76:79], v[180:183], v[184:187]\n"
    "v_exp_f32 v211, v211\n"
    "v_mfma_f32_16x16x32_bf16 v[188:191], v[80:83], v[180:183], v[188:191]\n"
    "ds_read_b128 v[124:127], v1 offset:55424\n"
    "v_exp_f32 v212, v212\n"
    "v_mfma_f32_16x16x32_bf16 v[192:195], v[84:87], v[180:183], v[192:195]\n"
    "v_exp_f32 v213, v213\n"
    "v_mfma_f32_16x16x32_bf16 v[196:199], v[16:19], v[88:91], 0\n"
    "v_exp_f32 v214, v214\n"
    "v_mfma_f32_16x16x32_bf16 v[200:203], v[20:23], v[88:91], 0\n"
    "v_exp_f32 v215, v215\n"
    "v_mfma_f32_16x16x32_bf16 v[204:207], v[24:27], v[88:91], 0\n"
    "ds_read_b128 v[128:131], v0 offset:55552\n"
    "v_exp_f32 v216, v216\n"
    "v_mfma_f32_16x16x32_bf16 v[196:199], v[28:31], v[92:95], v[196:199]\n"
    "v_cvt_pk_bf16_f32 v184, v184, v185\n"
    "v_cvt_pk_bf16_f32 v185, v186, v187\n"
    "v_cvt_pk_bf16_f32 v186, v188, v189\n"
    "v_mfma_f32_16x16x32_bf16 v[200:203], v[32:35], v[92:95], v[200:203]\n"
    "v_cvt_pk_bf16_f32 v187, v190, v191\n"
    "ds_write_b128 v2, v[184:187] offset:43008\n"
    "v_mfma_f32_16x16x32_bf16 v[204:207], v[36:39], v[92:95], v[204:207]\n"
    "v_cvt_pk_bf16_f32 v192, v192, v193\n"
    "v_cvt_pk_bf16_f32 v193, v194, v195\n"
    "v_mfma_f32_16x16x32_bf16 v[196:199], v[40:43], v[96:99], v[196:199]\n"
    "ds_read_b128 v[132:135], v1 offset:55552\n"
    "ds_write_b64 v3, v[192:193] offset:43008\n"
    "v_mfma_f32_16x16x32_bf16 v[200:203], v[44:47], v[96:99], v[200:203]\n"
    "v_exp_f32 v217, v217\n"
    "v_mfma_f32_16x16x32_bf16 v[204:207], v[48:51], v[96:99], v[204:207]\n"
    "v_exp_f32 v218, v218\n"
    "v_mfma_f32_16x16x32_bf16 v[196:199], v[52:55], v[100:103], v[196:199]\n"
    "v_exp_f32 v219, v219\n"
    "v_mfma_f32_16x16x32_bf16 v[200:203], v[56:59], v[100:103], v[200:203]\n"
    "v_exp_f32 v220, v220\n"
    "v_mfma_f32_16x16x32_bf16 v[204:207], v[60:63], v[100:103], v[204:207]\n"
    "v_exp_f32 v221, v221\n"
    "v_cvt_pk_bf16_f32 v224, v212, v213\n"
    "v_mfma_f32_16x16x32_bf16 v[196:199], v[64:67], v[104:107], v[196:199]\n"
    "v_cvt_pk_bf16_f32 v225, v214, v215\n"
    "v_cvt_pk_bf16_f32 v226, v216, v217\n"
    "v_cvt_pk_bf16_f32 v227, v218, v219\n"
    "v_mfma_f32_16x16x32_bf16 v[200:203], v[68:71], v[104:107], v[200:203]\n"
    "v_cvt_pk_bf16_f32 v228, v220, v221\n"
    "v_mov_b32 v229, 0\n"
    "v_cvt_pk_bf16_f32 v230, v208, v209\n"
    "v_mfma_f32_16x16x32_bf16 v[204:207], v[72:75], v[104:107], v[204:207]\n"
    "v_cvt_pk_bf16_f32 v231, v210, v211\n"
    "v_mfma_f32_16x16x32_bf16 v[196:199], v[76:79], v[108:111], v[196:199]\n"
    "v_mfma_f32_16x16x32_bf16 v[232:235], v[236:239], v[224:227], 0\n"
    "v_mfma_f32_16x16x32_bf16 v[232:235], v[236:239], v[228:231], v[232:235]\n"
    "v_mfma_f32_16x16x32_bf16 v[200:203], v[80:83], v[108:111], v[200:203]\n"
    "v_mfma_f32_16x16x32_bf16 v[204:207], v[84:87], v[108:111], v[204:207]\n"
    "v_mfma_f32_16x16x32_bf16 v[184:187], v[16:19], v[112:115], 0\n"
    "v_mfma_f32_16x16x32_bf16 v[188:191], v[20:23], v[112:115], 0\n"
    "s_nop 3\n"
    "v_rcp_f32 v250, v232\n"
    "s_waitcnt lgkmcnt(0)\n"
    "v_mfma_f32_16x16x32_bf16 v[184:187], v[28:31], v[116:119], v[184:187]\n"
    "v_cndmask_b32 v250, 0, v250, s[76:77]\n"
    "v_mul_f32 v208, v208, v250\n"
    "v_mul_f32 v209, v209, v250\n"
    "v_mfma_f32_16x16x32_bf16 v[188:191], v[32:35], v[116:119], v[188:191]\n"
    "v_cvt_pk_bf16_f32 v196, v196, v197\n"
    "v_cvt_pk_bf16_f32 v197, v198, v199\n"
    "v_cvt_pk_bf16_f32 v198, v200, v201\n"
    "v_mfma_f32_16x16x32_bf16 v[184:187], v[40:43], v[120:123], v[184:187]\n"
    "v_cvt_pk_bf16_f32 v199, v202, v203\n"
    "ds_write_b128 v2, v[196:199] offset:0\n"
    "v_mfma_f32_16x16x32_bf16 v[188:191], v[44:47], v[120:123], v[188:191]\n"
    "v_cvt_pk_bf16_f32 v204, v204, v205\n"
    "v_cvt_pk_bf16_f32 v205, v206, v207\n"
    "v_mul_f32 v210, v210, v250\n"
    "v_mfma_f32_16x16x32_bf16 v[184:187], v[52:55], v[124:127], v[184:187]\n"
    "ds_write_b64 v3, v[204:205] offset:0\n"
    "v_mul_f32 v211, v211, v250\n"
    "v_mfma_f32_16x16x32_bf16 v[188:191], v[56:59], v[124:127], v[188:191]\n"
    "v_mul_f32 v212, v212, v250\n"
    "v_mul_f32 v213, v213, v250\n"
    "v_mul_f32 v214, v214, v250\n"
    "v_mfma_f32_16x16x32_bf16 v[184:187], v[64:67], v[128:131], v[184:187]\n"
    "v_mul_f32 v215, v215, v250\n"
    "v_mul_f32 v216, v216, v250\n"
    "v_mul_f32 v217, v217, v250\n"
    "v_mfma_f32_16x16x32_bf16 v[188:191], v[68:71], v[128:131], v[188:191]\n"
    "v_mul_f32 v218, v218, v250\n"
    "v_mul_f32 v219, v219, v250\n"
    "v_mul_f32 v220, v220, v250\n"
    "v_mfma_f32_16x16x32_bf16 v[184:187], v[76:79], v[132:135], v[184:187]\n"
    "v_mul_f32 v221, v221, v250\n"
    "v_cvt_pk_bf16_f32 v224, v212, v213\n"
    "v_cvt_pk_bf16_f32 v225, v214, v215\n"
    "v_mfma_f32_16x16x32_bf16 v[188:191], v[80:83], v[132:135], v[188:191]\n"
    "v_cvt_pk_bf16_f32 v226, v216, v217\n"
    "v_cvt_pk_bf16_f32 v227, v218, v219\n"
    "v_mov_b32 v241, v7\n"
    "v_mfma_f32_16x16x32_bf16 v[192:195], v[24:27], v[112:115], 0\n"
    "ds_write_b128 v4, v[224:227] offset:8192\n"
    "v_cvt_pk_bf16_f32 v228, v220, v221\n"
    "v_mfma_f32_16x16x32_bf16 v[192:195], v[36:39], v[116:119], v[192:195]\n"
    "v_cvt_pk_bf16_f32 v230, v208, v209\n"
    "v_cvt_pk_bf16_f32 v231, v210, v211\n"
    "v_mfma_f32_16x16x32_bf16 v[192:195], v[48:51], v[120:123], v[192:195]\n"
    "v_cvt_pk_bf16_f32 v184, v184, v185\n"
    "v_cvt_pk_bf16_f32 v185, v186, v187\n"
    "v_cvt_pk_bf16_f32 v186, v188, v189\n"
    "v_mfma_f32_16x16x32_bf16 v[192:195], v[60:63], v[124:127], v[192:195]\n"
    "v_cvt_pk_bf16_f32 v187, v190, v191\n"
    "ds_write_b128 v2, v[184:187] offset:6144\n"
    "v_mfma_f32_16x16x32_bf16 v[192:195], v[72:75], v[128:131], v[192:195]\n"
    "ds_write_b128 v4, v[228:231] offset:9216\n"
    "v_mfma_f32_16x16x32_bf16 v[192:195], v[84:87], v[132:135], v[192:195]\n"
    "s_add_u32 s60, s49, 6\n"
    "s_and_b32 s60, s60, 7\n"
    "s_mul_i32 s60, s60, 0x1800\n"
    "v_add_u32 v11, s60, v5\n"
    "s_cmp_eq_u32 s49, 0\n"
    "s_cselect_b32 s74, 0xfffcfffc, -1\n"
    "s_cmp_eq_u32 s49, 3\n"
    "s_cselect_b32 s74, 0x70007, s74\n"
    "s_mov_b32 s75, s74\n"
    "v_cndmask_b32 v11, v9, v11, s[74:75]\n"
    "v_cvt_pk_bf16_f32 v192, v192, v193\n"
    "v_cvt_pk_bf16_f32 v193, v194, v195\n"
    "ds_write_b64 v3, v[192:193] offset:6144\n"
    "s_waitcnt lgkmcnt(0)\n"
    "s_barrier\n"
    "ds_read_b128 v[184:187], v11 offset:0\n"
    "ds_read_b128 v[188:191], v11 offset:1024\n"
    "ds_read_b128 v[192:195], v11 offset:2048\n"
    "ds_read_b128 v[196:199], v11 offset:3072\n"
    "ds_read_b128 v[200:203], v11 offset:4096\n"
    "ds_read_b128 v[204:207], v11 offset:5120\n"
    "s_waitcnt lgkmcnt(5)\n"
    "v_mfma_f32_16x16x32_bf16 v[208:211], v[136:139], v[184:187], v[240:243]\n"
    "v_mfma_f32_16x16x32_bf16 v[212:215], v[160:163], v[184:187], 0\n"
    "v_mfma_f32_16x16x32_bf16 v[216:219], v[88:91], v[184:187], 0\n"
    "v_mfma_f32_16x16x32_bf16 v[220:223], v[112:115], v[184:187], v[244:247]\n"
    "s_waitcnt lgkmcnt(4)\n"
    "v_mfma_f32_16x16x32_bf16 v[208:211], v[140:143], v[188:191], v[208:211]\n"
    "ds_read_b128 v[136:139], v12 offset:0\n"
    "v_mfma_f32_16x16x32_bf16 v[212:215], v[164:167], v[188:191], v[212:215]\n"
    "v_mfma_f32_16x16x32_bf16 v[216:219], v[92:95], v[188:191], v[216:219]\n"
    "ds_read_b128 v[160:163], v12 offset:6144\n"
    "v_mfma_f32_16x16x32_bf16 v[220:223], v[116:119], v[188:191], v[220:223]\n"
    "s_waitcnt lgkmcnt(2)\n"
    "v_mfma_f32_16x16x32_bf16 v[208:211], v[144:147], v[192:195], v[208:211]\n"
    "ds_read_b128 v[140:143], v13 offset:0\n"
    "v_mfma_f32_16x16x32_bf16 v[212:215], v[168:171], v[192:195], v[212:215]\n"
    "v_mfma_f32_16x16x32_bf16 v[216:219], v[96:99], v[192:195], v[216:219]\n"
    "ds_read_b128 v[164:167], v13 offset:6144\n"
    "v_mfma_f32_16x16x32_bf16 v[220:223], v[120:123], v[192:195], v[220:223]\n"
    "v_mfma_f32_16x16x32_bf16 v[208:211], v[148:151], v[196:199], v[208:211]\n"
    "ds_read_b128 v[144:147], v12 offset:128\n"
    "v_mfma_f32_16x16x32_bf16 v[212:215], v[172:175], v[196:199], v[212:215]\n"
    "v_mfma_f32_16x16x32_bf16 v[216:219], v[100:103], v[196:199], v[216:219]\n"
    "ds_read_b128 v[168:171], v12 offset:6272\n"
    "v_mfma_f32_16x16x32_bf16 v[220:223], v[124:127], v[196:199], v[220:223]\n"
    "v_mfma_f32_16x16x32_bf16 v[208:211], v[152:155], v[200:203], v[208:211]\n"
    "ds_read_b128 v[148:151], v13 offset:128\n"
    "v_mfma_f32_16x16x32_bf16 v[212:215], v[176:179], v[200:203], v[212:215]\n"
    "v_mfma_f32_16x16x32_bf16 v[216:219], v[104:107], v[200:203], v[216:219]\n"
    "ds_read_b128 v[172:175], v13 offset:6272\n"
    "v_mfma_f32_16x16x32_bf16 v[220:223], v[128:131], v[200:203], v[220:223]\n"
    "v_mfma_f32_16x16x32_bf16 v[208:211], v[156:159], v[204:207], v[208:211]\n"
    "ds_read_b128 v[152:155], v12 offset:256\n"
    "v_mfma_f32_16x16x32_bf16 v[212:215], v[180:183], v[204:207], v[212:215]\n"
    "v_mfma_f32_16x16x32_bf16 v[216:219], v[108:111], v[204:207], v[216:219]\n"
    "ds_read_b128 v[176:179], v12 offset:6400\n"
    "v_mfma_f32_16x16x32_bf16 v[220:223], v[132:135], v[204:207], v[220:223]\n"
    "s_waitcnt lgkmcnt(3)\n"
    "v_mfma_f32_16x16x32_bf16 v[184:187], v[16:19], v[136:139], 0\n"
    "ds_read_b128 v[156:159], v13 offset:256\n"
    "v_mfma_f32_16x16x32_bf16 v[188:191], v[20:23], v[136:139], 0\n"
    "ds_read_b128 v[180:183], v13 offset:6400\n"
    "v_mfma_f32_16x16x32_bf16 v[192:195], v[24:27], v[136:139], 0\n"
    "v_max3_f32 v224, v208, v209, v210\n"
    "v_max3_f32 v225, v211, v212, v213\n"
    "v_max3_f32 v226, v214, v215, v216\n"
    "v_mfma_f32_16x16x32_bf16 v[184:187], v[28:31], v[140:143], v[184:187]\n"
    "v_max3_f32 v227, v217, v218, v219\n"
    "v_max3_f32 v228, v220, v221, v222\n"
    "v_max3_f32 v229, v224, v225, v226\n"
    "v_mfma_f32_16x16x32_bf16 v[188:191], v[32:35], v[140:143], v[188:191]\n"
    "ds_read_b128 v[88:91], v12 offset:12288\n"
    "v_max3_f32 v230, v229, v227, v228\n"
    "v_mov_b32 v249, v230\n"
    "v_mfma_f32_16x16x32_bf16 v[192:195], v[36:39], v[140:143], v[192:195]\n"
    "s_nop 0\n"
    "v_permlane16_swap_b32 v230, v249\n"
    "v_max_f32 v248, v230, v249\n"
    "v_mov_b32 v249, v248\n"
    "v_mfma_f32_16x16x32_bf16 v[184:187], v[40:43], v[144:147], v[184:187]\n"
    "v_mfma_f32_16x16x32_bf16 v[188:191], v[44:47], v[144:147], v[188:191]\n"
    "v_permlane32_swap_b32 v248, v249\n"
    "v_max_f32 v248, v248, v249\n"
    "v_sub_f32 v208, v208, v248\n"
    "v_mfma_f32_16x16x32_bf16 v[192:195], v[48:51], v[144:147], v[192:195]\n"
    "ds_read_b128 v[92:95], v13 offset:12288\n"
    "v_sub_f32 v209, v209, v248\n"
    "v_sub_f32 v210, v210, v248\n"
    "v_sub_f32 v211, v211, v248\n"
    "v_mfma_f32_16x16x32_bf16 v[184:187], v[52:55], v[148:151], v[184:187]\n"
    "v_sub_f32 v212, v212, v248\n"
    "v_sub_f32 v213, v213, v248\n"
    "v_sub_f32 v214, v214, v248\n"
    "v_mfma_f32_16x16x32_bf16 v[188:191], v[56:59], v[148:151], v[188:191]\n"
    "v_sub_f32 v215, v215, v248\n"
    "v_sub_f32 v216, v216, v248\n"
    "v_sub_f32 v217, v217, v248\n"
    "v_mfma_f32_16x16x32_bf16 v[192:195], v[60:63], v[148:151], v[192:195]\n"
    "v_sub_f32 v218, v218, v248\n"
    "v_sub_f32 v219, v219, v248\n"
    "v_sub_f32 v220, v220, v248\n"
    "s_waitcnt lgkmcnt(1)\n"
    "v_mfma_f32_16x16x32_bf16 v[184:187], v[64:67], v[152:155], v[184:187]\n"
    "ds_read_b128 v[96:99], v12 offset:12416\n"
    "v_sub_f32 v221, v221, v248\n"
    "v_sub_f32 v222, v222, v248\n"
    "v_mfma_f32_16x16x32_bf16 v[188:191], v[68:71], v[152:155], v[188:191]\n"
    "v_exp_f32 v208, v208\n"
    "v_mfma_f32_16x16x32_bf16 v[192:195], v[72:75], v[152:155], v[192:195]\n"
    "v_exp_f32 v209, v209\n"
    "v_mfma_f32_16x16x32_bf16 v[184:187], v[76:79], v[156:159], v[184:187]\n"
    "v_exp_f32 v210, v210\n"
    "v_mfma_f32_16x16x32_bf16 v[188:191], v[80:83], v[156:159], v[188:191]\n"
    "ds_read_b128 v[100:103], v13 offset:12416\n"
    "v_exp_f32 v211, v211\n"
    "v_mfma_f32_16x16x32_bf16 v[192:195], v[84:87], v[156:159], v[192:195]\n"
    "v_exp_f32 v212, v212\n"
    "v_mfma_f32_16x16x32_bf16 v[196:199], v[16:19], v[160:163], 0\n"
    "v_exp_f32 v213, v213\n"
    "v_mfma_f32_16x16x32_bf16 v[200:203], v[20:23], v[160:163], 0\n"
    "v_exp_f32 v214, v214\n"
    "v_mfma_f32_16x16x32_bf16 v[204:207], v[24:27], v[160:163], 0\n"
    "ds_read_b128 v[104:107], v12 offset:12544\n"
    "v_exp_f32 v215, v215\n"
    "v_mfma_f32_16x16x32_bf16 v[196:199], v[28:31], v[164:167], v[196:199]\n"
    "v_cvt_pk_bf16_f32 v184, v184, v185\n"
    "v_cvt_pk_bf16_f32 v185, v186, v187\n"
    "v_cvt_pk_bf16_f32 v186, v188, v189\n"
    "v_mfma_f32_16x16x32_bf16 v[200:203], v[32:35], v[164:167], v[200:203]\n"
    "v_cvt_pk_bf16_f32 v187, v190, v191\n"
    "ds_write_b128 v2, v[184:187] offset:12288\n"
    "v_mfma_f32_16x16x32_bf16 v[204:207], v[36:39], v[164:167], v[204:207]\n"
    "v_cvt_pk_bf16_f32 v192, v192, v193\n"
    "v_cvt_pk_bf16_f32 v193, v194, v195\n"
    "v_mfma_f32_16x16x32_bf16 v[196:199], v[40:43], v[168:171], v[196:199]\n"
    "ds_read_b128 v[108:111], v13 offset:12544\n"
    "ds_write_b64 v3, v[192:193] offset:12288\n"
    "v_mfma_f32_16x16x32_bf16 v[200:203], v[44:47], v[168:171], v[200:203]\n"
    "v_exp_f32 v216, v216\n"
    "v_mfma_f32_16x16x32_bf16 v[204:207], v[48:51], v[168:171], v[204:207]\n"
    "v_exp_f32 v217, v217\n"
    "v_mfma_f32_16x16x32_bf16 v[196:199], v[52:55], v[172:175], v[196:199]\n"
    "v_exp_f32 v218, v218\n"
    "v_mfma_f32_16x16x32_bf16 v[200:203], v[56:59], v[172:175], v[200:203]\n"
    "v_exp_f32 v219, v219\n"
    "v_mfma_f32_16x16x32_bf16 v[204:207], v[60:63], v[172:175], v[204:207]\n"
    "v_exp_f32 v220, v220\n"
    "v_mfma_f32_16x16x32_bf16 v[196:199], v[64:67], v[176:179], v[196:199]\n"
    "v_exp_f32 v221, v221\n"
    "v_mfma_f32_16x16x32_bf16 v[200:203], v[68:71], v[176:179], v[200:203]\n"
    "v_exp_f32 v222, v222\n"
    "v_cvt_pk_bf16_f32 v224, v216, v217\n"
    "v_mfma_f32_16x16x32_bf16 v[204:207], v[72:75], v[176:179], v[204:207]\n"
    "v_cvt_pk_bf16_f32 v225, v218, v219\n"
    "v_cvt_pk_bf16_f32 v226, v220, v221\n"
    "v_cvt_pk_bf16_f32 v227, v222, 0\n"
    "v_mfma_f32_16x16x32_bf16 v[196:199], v[76:79], v[180:183], v[196:199]\n"
    "v_cvt_pk_bf16_f32 v228, v208, v209\n"
    "v_cvt_pk_bf16_f32 v229, v210, v211\n"
    "v_cvt_pk_bf16_f32 v230, v212, v213\n"
    "v_mfma_f32_16x16x32_bf16 v[200:203], v[80:83], v[180:183], v[200:203]\n"
    "v_cvt_pk_bf16_f32 v231, v214, v215\n"
    "v_mfma_f32_16x16x32_bf16 v[204:207], v[84:87], v[180:183], v[204:207]\n"
    "v_mfma_f32_16x16x32_bf16 v[232:235], v[236:239], v[224:227], 0\n"
    "v_mfma_f32_16x16x32_bf16 v[232:235], v[236:239], v[228:231], v[232:235]\n"
    "v_mfma_f32_16x16x32_bf16 v[184:187], v[16:19], v[88:91], 0\n"
    "v_mfma_f32_16x16x32_bf16 v[188:191], v[20:23], v[88:91], 0\n"
    "s_waitcnt lgkmcnt(0)\n"
    "v_mfma_f32_16x16x32_bf16 v[184:187], v[28:31], v[92:95], v[184:187]\n"
    "v_mfma_f32_16x16x32_bf16 v[188:191], v[32:35], v[92:95], v[188:191]\n"
    "v_cvt_pk_bf16_f32 v196, v196, v197\n"
    "v_cvt_pk_bf16_f32 v197, v198, v199\n"
    "v_cvt_pk_bf16_f32 v198, v200, v201\n"
    "v_mfma_f32_16x16x32_bf16 v[184:187], v[40:43], v[96:99], v[184:187]\n"
    "v_cvt_pk_bf16_f32 v199, v202, v203\n"
    "ds_write_b128 v2, v[196:199] offset:18432\n"
    "v_mfma_f32_16x16x32_bf16 v[188:191], v[44:47], v[96:99], v[188:191]\n"
    "v_cvt_pk_bf16_f32 v204, v204, v205\n"
    "v_cvt_pk_bf16_f32 v205, v206, v207\n"
    "v_mfma_f32_16x16x32_bf16 v[184:187], v[52:55], v[100:103], v[184:187]\n"
    "ds_write_b64 v3, v[204:205] offset:18432\n"
    "v_mfma_f32_16x16x32_bf16 v[188:191], v[56:59], v[100:103], v[188:191]\n"
    "v_rcp_f32 v250, v232\n"
    "v_mfma_f32_16x16x32_bf16 v[184:187], v[64:67], v[104:107], v[184:187]\n"
    "v_cndmask_b32 v250, 0, v250, s[74:75]\n"
    "v_mul_f32 v208, v208, v250\n"
    "v_mul_f32 v209, v209, v250\n"
    "v_mfma_f32_16x16x32_bf16 v[188:191], v[68:71], v[104:107], v[188:191]\n"
    "v_mul_f32 v210, v210, v250\n"
    "v_mul_f32 v211, v211, v250\n"
    "v_mul_f32 v212, v212, v250\n"
    "v_mfma_f32_16x16x32_bf16 v[184:187], v[76:79], v[108:111], v[184:187]\n"
    "v_mul_f32 v213, v213, v250\n"
    "v_mul_f32 v214, v214, v250\n"
    "v_mul_f32 v215, v215, v250\n"
    "v_mfma_f32_16x16x32_bf16 v[188:191], v[80:83], v[108:111], v[188:191]\n"
    "v_mul_f32 v216, v216, v250\n"
    "v_mul_f32 v217, v217, v250\n"
    "v_mul_f32 v218, v218, v250\n"
    "v_mfma_f32_16x16x32_bf16 v[192:195], v[24:27], v[88:91], 0\n"
    "v_mul_f32 v219, v219, v250\n"
    "v_mul_f32 v220, v220, v250\n"
    "v_mul_f32 v221, v221, v250\n"
    "v_mfma_f32_16x16x32_bf16 v[192:195], v[36:39], v[92:95], v[192:195]\n"
    "v_mul_f32 v222, v222, v250\n"
    "v_cvt_pk_bf16_f32 v224, v216, v217\n"
    "v_cvt_pk_bf16_f32 v225, v218, v219\n"
    "v_mfma_f32_16x16x32_bf16 v[192:195], v[48:51], v[96:99], v[192:195]\n"
    "v_cvt_pk_bf16_f32 v184, v184, v185\n"
    "v_cvt_pk_bf16_f32 v185, v186, v187\n"
    "v_cvt_pk_bf16_f32 v186, v188, v189\n"
    "v_mfma_f32_16x16x32_bf16 v[192:195], v[60:63], v[100:103], v[192:195]\n"
    "v_cvt_pk_bf16_f32 v187, v190, v191\n"
    "ds_write_b128 v2, v[184:187] offset:24576\n"
    "v_mfma_f32_16x16x32_bf16 v[192:195], v[72:75], v[104:107], v[192:195]\n"
    "v_cvt_pk_bf16_f32 v226, v220, v221\n"
    "v_cvt_pk_bf16_f32 v227, v222, 0\n"
    "v_mov_b32 v242, v7\n"
    "v_mfma_f32_16x16x32_bf16 v[192:195], v[84:87], v[108:111], v[192:195]\n"
    "ds_write_b128 v4, v[224:227] offset:0\n"
    "v_cvt_pk_bf16_f32 v228, v208, v209\n"
    "v_cvt_pk_bf16_f32 v229, v210, v211\n"
    "v_cvt_pk_bf16_f32 v230, v212, v213\n"
    "v_cvt_pk_bf16_f32 v231, v214, v215\n"
    "ds_write_b128 v4, v[228:231] offset:1024\n"
    "s_add_u32 s60, s49, 9\n"
    "s_and_b32 s60, s60, 7\n"
    "s_mul_i32 s60, s60, 0x1800\n"
    "v_add_u32 v11, s60, v5\n"
    "s_cmp_eq_u32 s49, 0\n"
    "s_cselect_b32 s76, 0xfff8fff8, -1\n"
    "s_cmp_eq_u32 s49, 3\n"
    "s_cselect_b32 s76, 0xf000f, s76\n"
    "s_mov_b32 s77, s76\n"
    "v_cndmask_b32 v11, v9, v11, s[76:77]\n"
    "v_cvt_pk_bf16_f32 v192, v192, v193\n"
    "v_cvt_pk_bf16_f32 v193, v194, v195\n"
    "ds_write_b64 v3, v[192:193] offset:24576\n"
    "s_waitcnt lgkmcnt(0)\n"
    "s_barrier\n"
    "ds_read_b128 v[184:187], v11 offset:0\n"
    "ds_read_b128 v[188:191], v11 offset:1024\n"
    "ds_read_b128 v[192:195], v11 offset:2048\n"
    "ds_read_b128 v[196:199], v11 offset:3072\n"
    "ds_read_b128 v[200:203], v11 offset:4096\n"
    "ds_read_b128 v[204:207], v11 offset:5120\n"
    "s_waitcnt lgkmcnt(5)\n"
    "v_mfma_f32_16x16x32_bf16 v[208:211], v[112:115], v[184:187], v[240:243]\n"
    "v_mfma_f32_16x16x32_bf16 v[212:215], v[136:139], v[184:187], 0\n"
    "v_mfma_f32_16x16x32_bf16 v[216:219], v[160:163], v[184:187], 0\n"
    "v_mfma_f32_16x16x32_bf16 v[220:223], v[88:91], v[184:187], v[244:247]\n"
    "s_waitcnt lgkmcnt(4)\n"
    "v_mfma_f32_16x16x32_bf16 v[208:211], v[116:119], v[188:191], v[208:211]\n"
    "v_mfma_f32_16x16x32_bf16 v[212:215], v[140:143], v[188:191], v[212:215]\n"
    "v_mfma_f32_16x16x32_bf16 v[216:219], v[164:167], v[188:191], v[216:219]\n"
    "v_mfma_f32_16x16x32_bf16 v[220:223], v[92:95], v[188:191], v[220:223]\n"
    "s_waitcnt lgkmcnt(0)\n"
    "v_mfma_f32_16x16x32_bf16 v[208:211], v[120:123], v[192:195], v[208:211]\n"
    "v_mfma_f32_16x16x32_bf16 v[212:215], v[144:147], v[192:195], v[212:215]\n"
    "v_mfma_f32_16x16x32_bf16 v[216:219], v[168:171], v[192:195], v[216:219]\n"
    "v_mfma_f32_16x16x32_bf16 v[220:223], v[96:99], v[192:195], v[220:223]\n"
    "v_mfma_f32_16x16x32_bf16 v[208:211], v[124:127], v[196:199], v[208:211]\n"
    "v_mfma_f32_16x16x32_bf16 v[212:215], v[148:151], v[196:199], v[212:215]\n"
    "v_mfma_f32_16x16x32_bf16 v[216:219], v[172:175], v[196:199], v[216:219]\n"
    "v_mfma_f32_16x16x32_bf16 v[220:223], v[100:103], v[196:199], v[220:223]\n"
    "v_mfma_f32_16x16x32_bf16 v[208:211], v[128:131], v[200:203], v[208:211]\n"
    "v_mfma_f32_16x16x32_bf16 v[212:215], v[152:155], v[200:203], v[212:215]\n"
    "v_mfma_f32_16x16x32_bf16 v[216:219], v[176:179], v[200:203], v[216:219]\n"
    "v_mfma_f32_16x16x32_bf16 v[220:223], v[104:107], v[200:203], v[220:223]\n"
    "v_mfma_f32_16x16x32_bf16 v[208:211], v[132:135], v[204:207], v[208:211]\n"
    "v_mfma_f32_16x16x32_bf16 v[212:215], v[156:159], v[204:207], v[212:215]\n"
    "v_mfma_f32_16x16x32_bf16 v[216:219], v[180:183], v[204:207], v[216:219]\n"
    "v_mfma_f32_16x16x32_bf16 v[220:223], v[108:111], v[204:207], v[220:223]\n"
    "s_nop 4\n"
    "v_max3_f32 v224, v208, v209, v210\n"
    "v_max3_f32 v225, v211, v212, v213\n"
    "v_max3_f32 v226, v214, v215, v216\n"
    "v_max3_f32 v227, v217, v218, v219\n"
    "v_max3_f32 v228, v220, v221, v222\n"
    "v_max3_f32 v229, v224, v225, v226\n"
    "v_max3_f32 v230, v227, v228, v223\n"
    "v_max_f32 v231, v229, v230\n"
    "v_mov_b32 v249, v231\n"
    "s_nop 1\n"
    "v_permlane16_swap_b32 v231, v249\n"
    "v_max_f32 v248, v231, v249\n"
    "v_mov_b32 v249, v248\n"
    "s_nop 1\n"
    "v_permlane32_swap_b32 v248, v249\n"
    "v_max_f32 v248, v248, v249\n"
    "v_sub_f32 v208, v208, v248\n"
    "v_sub_f32 v209, v209, v248\n"
    "v_sub_f32 v210, v210, v248\n"
    "v_sub_f32 v211, v211, v248\n"
    "v_sub_f32 v212, v212, v248\n"
    "v_sub_f32 v213, v213, v248\n"
    "v_sub_f32 v214, v214, v248\n"
    "v_sub_f32 v215, v215, v248\n"
    "v_sub_f32 v216, v216, v248\n"
    "v_sub_f32 v217, v217, v248\n"
    "v_sub_f32 v218, v218, v248\n"
    "v_sub_f32 v219, v219, v248\n"
    "v_sub_f32 v220, v220, v248\n"
    "v_sub_f32 v221, v221, v248\n"
    "v_sub_f32 v222, v222, v248\n"
    "v_sub_f32 v223, v223, v248\n"
    "v_exp_f32 v208, v208\n"
    "v_exp_f32 v209, v209\n"
    "v_exp_f32 v210, v210\n"
    "v_exp_f32 v211, v211\n"
    "v_exp_f32 v212, v212\n"
    "v_exp_f32 v213, v213\n"
    "v_exp_f32 v214, v214\n"
    "v_exp_f32 v215, v215\n"
    "v_exp_f32 v216, v216\n"
    "v_exp_f32 v217, v217\n"
    "v_exp_f32 v218, v218\n"
    "v_exp_f32 v219, v219\n"
    "v_exp_f32 v220, v220\n"
    "v_exp_f32 v221, v221\n"
    "v_exp_f32 v222, v222\n"
    "v_exp_f32 v223, v223\n"
    "v_cvt_pk_bf16_f32 v224, v220, v221\n"
    "v_cvt_pk_bf16_f32 v225, v222, v223\n"
    "v_cvt_pk_bf16_f32 v226, v208, v209\n"
    "v_cvt_pk_bf16_f32 v227, v210, v211\n"
    "v_cvt_pk_bf16_f32 v228, v212, v213\n"
    "v_cvt_pk_bf16_f32 v229, v214, v215\n"
    "v_cvt_pk_bf16_f32 v230, v216, v217\n"
    "v_cvt_pk_bf16_f32 v231, v218, v219\n"
    "v_mfma_f32_16x16x32_bf16 v[232:235], v[236:239], v[224:227], 0\n"
    "s_nop 0\n"
    "v_mfma_f32_16x16x32_bf16 v[232:235], v[236:239], v[228:231], v[232:235]\n"
    "s_nop 7\n"
    "v_rcp_f32 v250, v232\n"
    "s_nop 0\n"
    "v_cndmask_b32 v250, 0, v250, s[76:77]\n"
    "v_mul_f32 v208, v208, v250\n"
    "v_mul_f32 v209, v209, v250\n"
    "v_mul_f32 v210, v210, v250\n"
    "v_mul_f32 v211, v211, v250\n"
    "v_mul_f32 v212, v212, v250\n"
    "v_mul_f32 v213, v213, v250\n"
    "v_mul_f32 v214, v214, v250\n"
    "v_mul_f32 v215, v215, v250\n"
    "v_mul_f32 v216, v216, v250\n"
    "v_mul_f32 v217, v217, v250\n"
    "v_mul_f32 v218, v218, v250\n"
    "v_mul_f32 v219, v219, v250\n"
    "v_mul_f32 v220, v220, v250\n"
    "v_mul_f32 v221, v221, v250\n"
    "v_mul_f32 v222, v222, v250\n"
    "v_mul_f32 v223, v223, v250\n"
    "v_cvt_pk_bf16_f32 v224, v220, v221\n"
    "v_cvt_pk_bf16_f32 v225, v222, v223\n"
    "v_cvt_pk_bf16_f32 v226, v208, v209\n"
    "v_cvt_pk_bf16_f32 v227, v210, v211\n"
    "ds_write_b128 v4, v[224:227] offset:8192\n"
    "v_cvt_pk_bf16_f32 v228, v212, v213\n"
    "v_cvt_pk_bf16_f32 v229, v214, v215\n"
    "v_cvt_pk_bf16_f32 v230, v216, v217\n"
    "v_cvt_pk_bf16_f32 v231, v218, v219\n"
    "ds_write_b128 v4, v[228:231] offset:9216\n"
    "ds_read_b128 v[16:19], v14 offset:0\n"
    "ds_read_b64 v[20:21], v14 offset:16\n"
    "s_waitcnt lgkmcnt(0)\n"
    "s_barrier\n"
    "ds_read_b128 v[24:27], v21 offset:37632\n"
    "ds_read_b128 v[28:31], v21 offset:41728\n"
    "ds_read_b128 v[32:35], v21 offset:45824\n"
    "ds_read_b128 v[36:39], v21 offset:49920\n"
    "ds_read_b128 v[40:43], v21 offset:54016\n"
    "s_waitcnt lgkmcnt(4)\n"
    "global_store_dwordx4 v16, v[24:27], s[50:51] offset:0 sc1\n"
    "s_waitcnt lgkmcnt(3)\n"
    "global_store_dwordx4 v17, v[28:31], s[50:51] offset:0 sc1\n"
    "s_waitcnt lgkmcnt(2)\n"
    "global_store_dwordx4 v18, v[32:35], s[50:51] offset:0 sc1\n"
    "s_waitcnt lgkmcnt(1)\n"
    "global_store_dwordx4 v19, v[36:39], s[50:51] offset:0 sc1\n"
    "s_mov_b64 exec, s[56:57]\n"
    "s_waitcnt lgkmcnt(0)\n"
    "global_store_dwordx4 v20, v[40:43], s[50:51] offset:0 sc1\n"
    "s_mov_b64 exec, -1\n"
    "v_add_u32 v22, 0x4000, v21\n"
    "s_barrier\n"
    "ds_read_b128 v[56:59], v22 offset:48256\n"
    "ds_read_b128 v[60:63], v22 offset:52352\n"
    "ds_read_b128 v[64:67], v22 offset:56448\n"
    "s_waitcnt lgkmcnt(2)\n"
    "global_store_dwordx4 v18, v[56:59], s[50:51] offset:2688 sc1\n"
    "s_waitcnt lgkmcnt(1)\n"
    "global_store_dwordx4 v19, v[60:63], s[50:51] offset:2688 sc1\n"
    "s_mov_b64 exec, s[56:57]\n"
    "s_waitcnt lgkmcnt(0)\n"
    "global_store_dwordx4 v20, v[64:67], s[50:51] offset:2688 sc1\n"
    "s_mov_b64 exec, -1\n"
    "s_endpgm\n"
    "Lend_%=:\n"
      :
      : "{s[0:1]}"(__builtin_amdgcn_kernarg_segment_ptr()), "{s2}"(blockIdx.x), "{v0}"(threadIdx.x),
        "s"((unsigned)(size_t)(__attribute__((address_space(3))) char*)smem)
      : "memory", "scc", "vcc", "v1", "v2", "v3", "v4", "v5", "v6", "v7", "v8", "v9", "v10", "v11", "v12", "v13", "v14", "v15", "v16", "v17", "v18", "v19", "v20", "v21", "v22", "v23", "v24", "v25", "v26", "v27", "v28", "v29", "v30", "v31", "v32", "v33", "v34", "v35", "v36", "v37", "v38", "v39", "v40", "v41", "v42", "v43", "v44", "v45", "v46", "v47", "v48", "v49", "v50", "v51", "v52", "v53", "v54", "v55", "v56", "v57", "v58", "v59", "v60", "v61", "v62", "v63", "v64", "v65", "v66", "v67", "v68", "v69", "v70", "v71", "v72", "v73", "v74", "v75", "v76", "v77", "v78", "v79", "v80", "v81", "v82", "v83", "v84", "v85", "v86", "v87", "v88", "v89", "v90", "v91", "v92", "v93", "v94", "v95", "v96", "v97", "v98", "v99", "v100", "v101", "v102", "v103", "v104", "v105", "v106", "v107", "v108", "v109", "v110", "v111", "v112", "v113", "v114", "v115", "v116", "v117", "v118", "v119", "v120", "v121", "v122", "v123", "v124", "v125", "v126", "v127", "v128", "v129", "v130", "v131", "v132", "v133", "v134", "v135", "v136", "v137", "v138", "v139", "v140", "v141", "v142", "v143", "v144", "v145", "v146", "v147", "v148", "v149", "v150", "v151", "v152", "v153", "v154", "v155", "v156", "v157", "v158", "v159", "v160", "v161", "v162", "v163", "v164", "v165", "v166", "v167", "v168", "v169", "v170", "v171", "v172", "v173", "v174", "v175", "v176", "v177", "v178", "v179", "v180", "v181", "v182", "v183", "v184", "v185", "v186", "v187", "v188", "v189", "v190", "v191", "v192", "v193", "v194", "v195", "v196", "v197", "v198", "v199", "v200", "v201", "v202", "v203", "v204", "v205", "v206", "v207", "v208", "v209", "v210", "v211", "v212", "v213", "v214", "v215", "v216", "v217", "v218", "v219", "v220", "v221", "v222", "v223", "v224", "v225", "v226", "v227", "v228", "v229", "v230", "v231", "v232", "v233", "v234", "v235", "v236", "v237", "v238", "v239", "v240", "v241", "v242", "v243", "v244", "v245", "v246", "v247", "v248", "v249", "v250", "v251", "v252", "v253", "v254", "v255", "s3", "s4", "s5", "s6", "s7", "s8", "s9", "s10", "s11", "s12", "s13", "s14", "s15", "s16", "s17", "s18", "s19", "s20", "s21", "s22", "s23", "s24", "s25", "s26", "s27", "s28", "s29", "s30", "s31", "s33", "s34", "s35", "s36", "s37", "s38", "s39", "s40", "s41", "s42", "s43", "s44", "s45", "s46", "s47", "s48", "s49", "s50", "s51", "s52", "s53", "s54", "s55", "s56", "s57", "s58", "s59", "s60", "s61", "s62", "s63", "s64", "s65", "s66", "s67", "s68", "s69", "s70", "s71", "s72", "s73", "s74", "s75", "s76", "s77", "s78", "s79", "s80", "s81", "s82", "s83", "s84", "s85", "s86", "s87", "s88", "s89", "s90", "s91");
}

extern "C" void kernel_launch(void* const* d_in, const int* in_sizes, int n_in, void* d_out, int out_size,
                              void* d_ws, size_t ws_size, hipStream_t stream) {
  attn_kernel<<<256, 512, 0, stream>>>((const char*)d_in[0], (const char*)d_in[1], (const char*)d_in[2],
                                       (char*)d_ws, (char*)d_out);
}
```
